```python
import torch

def _softmax(x, axis=-1):
    return torch.softmax(x, dim=axis)
def _gelu(x):
    return torch.nn.functional.gelu(x, approximate="tanh")
def _silu(x):
    return torch.nn.functional.silu(x)
def _sigmoid(x):
    return torch.sigmoid(x)
def _layernorm(x, axis=-1, eps=1e-5):
    m = x.mean(dim=axis, keepdim=True)
    v = x.var(dim=axis, keepdim=True, unbiased=False)
    return (x - m) / torch.sqrt(v + eps)
def _scatter_add(tgt, idx, src, axis=0):
    return tgt.clone().index_add_(axis, idx, src)
def _segment_sum(src, seg, n):
    out = torch.zeros((n,) + src.shape[1:], dtype=src.dtype)
    return out.index_add_(0, seg, src)
def _pad(x, pw):
    flat = tuple(v for pair in reversed(pw) for v in pair)
    return torch.nn.functional.pad(x, flat)

kernel_name = 'standalone__kernel_systems_refs_featgraph__graph'

def setup_inputs():

    _w = (lambda a: (lambda t: t.to(torch.bfloat16) if t.is_floating_point() else t)(torch.as_tensor(a)))
    N, deg, D = 12500, 32, 128
    E = N * deg
    scale = D ** -0.5
    src = (lambda lo, hi, *s: torch.randint(lo, hi, s))(0, N, E).to(torch.int64)
    dst = (lambda lo, hi, *s: torch.randint(lo, hi, s))(0, N, E).to(torch.int64)
    return {
        "x": _w((lambda *s: torch.randn(*s, dtype=torch.bfloat16) * 0.8)(N, D)),
        "src": _w(src),
        "dst": _w(dst),
        "w_q": _w((lambda *s: torch.randn(*s, dtype=torch.bfloat16) * 0.8)(D, D) * scale),
        "w_v": _w((lambda *s: torch.randn(*s, dtype=torch.bfloat16) * 0.05)(D, D) * scale),
    }

def reference(x, src, dst, w_q, w_v):
    N, D = x.shape
    xs = x[src]
    xd = x[dst]
    q = xd @ w_q
    logits = torch.einsum('ed,ed->e', xs, q) * (float(D) ** -0.5)
    alpha = _sigmoid(logits)
    msg = alpha[:, None] * (xs @ w_v)
    return _segment_sum(msg, dst, N)
```

```cpp
#include <hip/hip_runtime.h>
#include <hip/hip_bf16.h>
#include <stdint.h>

static __device__ __forceinline__ float bf2f(uint16_t v) { return __uint_as_float(((uint32_t)v) << 16); }
static __device__ __forceinline__ uint16_t f2bf(float f) { __hip_bfloat16 b = __float2bfloat16(f); return *(uint16_t*)&b; }

__global__ void k_q(const uint16_t* x, const uint16_t* w, uint16_t* q, int N) {
    int n = blockIdx.x; int d = threadIdx.x;
    float acc = 0.f;
    for (int k = 0; k < 128; ++k) acc += bf2f(x[n * 128 + k]) * bf2f(w[k * 128 + d]);
    q[n * 128 + d] = f2bf(acc);
}
__global__ void k_e(const uint16_t* x, const uint16_t* q, const int* src, const int* dst, float* agg, int E) {
    int e = blockIdx.x * 4 + (threadIdx.x >> 6); int l = threadIdx.x & 63;
    if (e >= E) return;
    int s = src[e], d = dst[e];
    float a0 = bf2f(x[s * 128 + l]), a1 = bf2f(x[s * 128 + 64 + l]);
    float p = a0 * bf2f(q[d * 128 + l]) + a1 * bf2f(q[d * 128 + 64 + l]);
    for (int o = 32; o > 0; o >>= 1) p += __shfl_xor(p, o);
    float al = 1.f / (1.f + __expf(-p * 0.08838834764831845f));
    atomicAdd(&agg[d * 128 + l], al * a0);
    atomicAdd(&agg[d * 128 + 64 + l], al * a1);
}
__global__ void k_o(const float* agg, const uint16_t* w, uint16_t* out, int N) {
    int n = blockIdx.x; int d = threadIdx.x;
    float acc = 0.f;
    for (int k = 0; k < 128; ++k) acc += agg[n * 128 + k] * bf2f(w[k * 128 + d]);
    out[n * 128 + d] = f2bf(acc);
}

extern "C" void kernel_launch(void* const* d_in, const int* in_sizes, int n_in,
                              void* d_out, int out_size, void* d_ws, size_t ws_size,
                              hipStream_t stream) {
    const uint16_t* x = (const uint16_t*)d_in[0];
    const int* src = (const int*)d_in[1];
    const int* dst = (const int*)d_in[2];
    const uint16_t* wq = (const uint16_t*)d_in[3];
    const uint16_t* wv = (const uint16_t*)d_in[4];
    int N = in_sizes[0] / 128, E = in_sizes[1];
    float* agg = (float*)d_ws;
    uint16_t* q = (uint16_t*)((char*)d_ws + (size_t)N * 128 * 4);
    hipMemsetAsync(agg, 0, (size_t)N * 128 * 4, stream);
    k_q<<<N, 128, 0, stream>>>(x, wq, q, N);
    k_e<<<(E + 3) / 4, 256, 0, stream>>>(x, q, src, dst, agg, E);
    k_o<<<N, 128, 0, stream>>>(agg, wv, (uint16_t*)d_out, N);
}
```

```cpp
#include <hip/hip_runtime.h>
#include <stdint.h>

typedef uint32_t u32;
typedef __attribute__((ext_vector_type(4))) uint32_t u32x4;
typedef __attribute__((ext_vector_type(2))) uint32_t u32x2;
typedef __attribute__((ext_vector_type(4))) float f32x4;
typedef __attribute__((ext_vector_type(4))) short s16x4;
typedef __attribute__((ext_vector_type(8))) __bf16 bf16x8;
typedef __attribute__((ext_vector_type(2))) __bf16 bf16x2;
#define LAS __attribute__((address_space(3)))

#define ST_DECL
#define ST(i)
#define ST_END(lo, hi)

#define PREFETCH 0

constexpr int NB = 256;
constexpr u32 DUMMY = 0x7FFFFF00u | 63u;

static __device__ __forceinline__ u32 dpp_scan_incl(u32 v) {
    v += __builtin_amdgcn_update_dpp(0u, v, 0x111, 0xf, 0xf, false);
    v += __builtin_amdgcn_update_dpp(0u, v, 0x112, 0xf, 0xf, false);
    v += __builtin_amdgcn_update_dpp(0u, v, 0x114, 0xf, 0xf, false);
    v += __builtin_amdgcn_update_dpp(0u, v, 0x118, 0xf, 0xf, false);
    v += __builtin_amdgcn_update_dpp(0u, v, 0x142, 0xa, 0xf, false);
    v += __builtin_amdgcn_update_dpp(0u, v, 0x143, 0xc, 0xf, false);
    return v;
}
static __device__ __forceinline__ u32 pk_bf16(float a, float b) {
    bf16x2 v = {(__bf16)a, (__bf16)b};
    return __builtin_bit_cast(u32, v);
}

__global__ __launch_bounds__(1024) void k_bin(const int* __restrict__ src, const int* __restrict__ dst,
                                              const char* __restrict__ x, u32* __restrict__ ent, u32* __restrict__ offs,
                                              int E, int CH, int xbytes
) {
    __shared__ __attribute__((aligned(16))) u32 hist[256];
    __shared__ __attribute__((aligned(16))) u32 start[256];
    __shared__ __attribute__((aligned(16))) u32 stage[2048];
    const int tid = threadIdx.x, p = blockIdx.x, lane = tid & 63;
    ST_DECL;
    ST(0);
    if (tid >= 512) {
        return;
    }
    const int e0 = p * CH + tid * 4;
    const int n = min(CH - tid * 4, E - e0);
    u32x4 d4 = {0, 0, 0, 0}, s4 = {0, 0, 0, 0};
    {
        const auto rd = __builtin_amdgcn_make_buffer_rsrc((void*)dst, 0, E * 4, 0x00020000);
        const auto rs = __builtin_amdgcn_make_buffer_rsrc((void*)src, 0, E * 4, 0x00020000);
        if (n > 0) {
            d4 = __builtin_amdgcn_raw_buffer_load_b128(rd, e0 * 4, 0, 0);
            s4 = __builtin_amdgcn_raw_buffer_load_b128(rs, e0 * 4, 0, 0);
        }
    }
    if (tid < 256) hist[tid] = 0;
    __syncthreads();
    u32 rank[4];
#pragma unroll
    for (int j = 0; j < 4; ++j) rank[j] = (j < n) ? atomicAdd(&hist[d4[j] & 255], 1u) : 0u;
    ST(1);
    __syncthreads();
    const u32x4 c = *(const u32x4*)&hist[lane * 4];
    const u32 tot = c[0] + c[1] + c[2] + c[3];
    const u32 ex = dpp_scan_incl(tot) - tot;
    const u32x4 st = {ex, ex + c[0], ex + c[0] + c[1], ex + c[0] + c[1] + c[2]};
    *(u32x4*)&start[lane * 4] = st;
    if (tid < 64) *(u32x4*)&offs[p * 256 + lane * 4] = st | (c << 16);
#pragma unroll
    for (int j = 0; j < 4; ++j)
        if (j < n) stage[start[d4[j] & 255] + rank[j]] = (s4[j] << 8) | (d4[j] >> 8);
    __syncthreads();
    if (n > 0) *(u32x4*)&ent[e0] = *(const u32x4*)&stage[tid * 4];
    ST(2);
    ST_END(0, 2);
}

constexpr int LIST_CAP = 8192 + 768;
constexpr int L_WV = 0, L_Q = 32768, L_SLOT = L_Q + 16384, L_XS = L_SLOT, L_LIST = L_SLOT + 128 * 512, L_WQ = L_LIST;
constexpr int L_CNT = L_LIST + LIST_CAP * 4, L_END = L_CNT + 256 + 16;

static __device__ __forceinline__ f32x4 mfma444(u32x2 a, u32x2 b, f32x4 c) {
    return __builtin_amdgcn_mfma_f32_4x4x4bf16_1k(__builtin_bit_cast(s16x4, a), __builtin_bit_cast(s16x4, b), c, 0, 0, 0);
}
template <int CTRL> static __device__ __forceinline__ float dppf(float v) {
    return __builtin_bit_cast(float, __builtin_amdgcn_update_dpp(0u, __builtin_bit_cast(u32, v), CTRL, 0xf, 0xf, true));
}
template <int CTRL> static __device__ __forceinline__ u32 dppu(u32 v) {
    return __builtin_amdgcn_update_dpp(0u, v, CTRL, 0xf, 0xf, true);
}
static __device__ __forceinline__ float diag4(f32x4 d) {
    float t = d[0] + dppf<0x55>(d[1]);
    t += dppf<0xAA>(d[2]);
    t += dppf<0xFF>(d[3]);
    return t;
}
struct Batch { u32x4 x0, x1, x2, x3, q; u32 node; };

static __device__ __forceinline__ void compute(const Batch& b, u32 m0, u32 m1, f32x4& acc0, f32x4& acc1) {
    const f32x4 z = {0.f, 0.f, 0.f, 0.f};
    const u32x2 ql = b.q.xy, qh = b.q.zw;
    f32x4 d0 = mfma444(b.x0.xy, ql, z), d1 = mfma444(b.x1.xy, ql, z), d2 = mfma444(b.x2.xy, ql, z), d3 = mfma444(b.x3.xy, ql, z);
    d0 = mfma444(b.x0.zw, qh, d0); d1 = mfma444(b.x1.zw, qh, d1); d2 = mfma444(b.x2.zw, qh, d2); d3 = mfma444(b.x3.zw, qh, d3);
    const float a = diag4(d0), bb = diag4(d1), c = diag4(d2), d = diag4(d3);
    float t0, t1, u;
    asm("s_nop 1\n\t"
        "v_add_f32_dpp %0, %3, %3 row_ror:8 row_mask:0xf bank_mask:0x3\n\t"
        "v_add_f32_dpp %1, %5, %5 row_ror:8 row_mask:0xf bank_mask:0x3\n\t"
        "v_add_f32_dpp %0, %4, %4 row_ror:8 row_mask:0xf bank_mask:0xc\n\t"
        "v_add_f32_dpp %1, %6, %6 row_ror:8 row_mask:0xf bank_mask:0xc\n\t"
        "s_nop 0\n\t"
        "v_add_f32_dpp %2, %0, %0 row_ror:12 row_mask:0xf bank_mask:0x5\n\t"
        "v_add_f32_dpp %2, %1, %1 row_ror:4 row_mask:0xf bank_mask:0xa"
        : "=&v"(t0), "=&v"(t1), "=&v"(u) : "v"(a), "v"(bb), "v"(c), "v"(d));
    const float ex = __builtin_amdgcn_exp2f(u * (-0.08838834764831845f * 1.4426950408889634f));
    const float al = __builtin_amdgcn_rcpf(1.0f + ex);
    const u32 a2 = pk_bf16(al, al);
    { const u32x2 A = {dppu<0x150>(a2) & m0, dppu<0x150>(a2) & m1}; acc0 = mfma444(A, b.x0.xy, acc0); acc1 = mfma444(A, b.x0.zw, acc1); }
    { const u32x2 A = {dppu<0x158>(a2) & m0, dppu<0x158>(a2) & m1}; acc0 = mfma444(A, b.x1.xy, acc0); acc1 = mfma444(A, b.x1.zw, acc1); }
    { const u32x2 A = {dppu<0x154>(a2) & m0, dppu<0x154>(a2) & m1}; acc0 = mfma444(A, b.x2.xy, acc0); acc1 = mfma444(A, b.x2.zw, acc1); }
    { const u32x2 A = {dppu<0x15C>(a2) & m0, dppu<0x15C>(a2) & m1}; acc0 = mfma444(A, b.x3.xy, acc0); acc1 = mfma444(A, b.x3.zw, acc1); }
}

static __device__ __forceinline__ void wgemm(const LAS char* wimg, int cg, int lane, const bf16x8 (&bf)[4], f32x4& o0, f32x4& o1) {
    const int kg = lane >> 4, q = (lane >> 2) & 3, p = lane & 3;
    const int swz = ((q >> 1) << 2) | ((kg & 1) << 3);
    const int base = (8 * kg + q) * 256 + (((4 * cg + p) ^ swz) << 4);
    const int h0 = (q & 1) << 3, h1 = h0 ^ 8;
    o0 = (f32x4){0.f, 0.f, 0.f, 0.f}; o1 = o0;
#pragma unroll
    for (int s = 0; s < 4; ++s) {
        const s16x4 a00 = __builtin_amdgcn_ds_read_tr16_b64_v4i16((LAS s16x4*)(wimg + base + h0 + s * 8192));
        const s16x4 a01 = __builtin_amdgcn_ds_read_tr16_b64_v4i16((LAS s16x4*)(wimg + base + h0 + s * 8192 + 1024));
        const s16x4 a10 = __builtin_amdgcn_ds_read_tr16_b64_v4i16((LAS s16x4*)(wimg + base + h1 + s * 8192));
        const s16x4 a11 = __builtin_amdgcn_ds_read_tr16_b64_v4i16((LAS s16x4*)(wimg + base + h1 + s * 8192 + 1024));
        const bf16x8 A0 = __builtin_bit_cast(bf16x8, __builtin_shufflevector(a00, a01, 0, 1, 2, 3, 4, 5, 6, 7));
        const bf16x8 A1 = __builtin_bit_cast(bf16x8, __builtin_shufflevector(a10, a11, 0, 1, 2, 3, 4, 5, 6, 7));
        o0 = __builtin_amdgcn_mfma_f32_16x16x32_bf16(A0, bf[s], o0, 0, 0, 0);
        o1 = __builtin_amdgcn_mfma_f32_16x16x32_bf16(A1, bf[s], o1, 0, 0, 0);
    }
}

__global__ __launch_bounds__(1024) void k_main(const char* __restrict__ x, const char* __restrict__ wq, const char* __restrict__ wv,
                                               const u32* __restrict__ ent, const u32* __restrict__ offs,
                                               char* __restrict__ out, int N, int CH
) {
    __shared__ __attribute__((aligned(16))) char smem_[L_END];
    LAS char* smem = (LAS char*)smem_;
    const int tid = threadIdx.x, lane = tid & 63;
    const int w = __builtin_amdgcn_readfirstlane(tid >> 6);
    const int b = ((blockIdx.x & 7) << 5) | (blockIdx.x >> 3);
    ST_DECL;
    ST(4);
    const auto rx = __builtin_amdgcn_make_buffer_rsrc((void*)x, 0, N * 256, 0x00020000);
    const auto re = __builtin_amdgcn_make_buffer_rsrc((void*)ent, 0, NB * CH * 4 + 256, 0x00020000);
    const int pp = tid >> 2, kk = tid & 3;
    const u32 oc = offs[pp * 256 + b];
    const int g = lane >> 4, i16 = lane & 15;
    const int lnode = 4 * w + g;
    const u32x4 xr = __builtin_amdgcn_raw_buffer_load_b128(rx, (lnode * 256 + b) * 256 + i16 * 16, 0, 0);
    u32x4 wqv[2], wvv[2];
#pragma unroll
    for (int j = 0; j < 2; ++j) {
        const int k = 8 * w + 2 * g + j;
        wqv[j] = *(const u32x4*)(wq + k * 256 + i16 * 16);
        wvv[j] = *(const u32x4*)(wv + k * 256 + i16 * 16);
    }
    if (tid < 65) *(LAS u32*)(smem + L_CNT + tid * 4) = 0;
    const int st = oc & 0xffff, cnt = oc >> 16;
    const int ebase = (pp * CH + st + kk * 4) * 4;
    u32x4 ea = __builtin_amdgcn_raw_buffer_load_b128(re, ebase, 0, 0), eb = {0, 0, 0, 0};
    if (cnt > 16 + kk * 4) eb = __builtin_amdgcn_raw_buffer_load_b128(re, ebase + 64, 0, 0);
    *(LAS u32x4*)(smem + L_XS + lnode * 256 + ((i16 ^ (lnode & 15)) << 4)) = xr;
#pragma unroll
    for (int j = 0; j < 2; ++j) {
        const int k = 8 * w + 2 * g + j;
        const int o = k * 256 + ((i16 ^ ((((k >> 1) & 1) << 2) | (((k >> 3) & 1) << 3))) << 4);
        *(LAS u32x4*)(smem + L_WQ + o) = j ? wqv[j].zwxy : wqv[j];
        *(LAS u32x4*)(smem + L_WV + o) = j ? wvv[j].zwxy : wvv[j];
    }
    __syncthreads();
    ST(5);
    const int nt = w & 3, cg = w >> 2;
    {
        bf16x8 bf[4];
#pragma unroll
        for (int s = 0; s < 4; ++s)
            bf[s] = *(const LAS bf16x8*)(smem + L_XS + (nt * 16 + i16) * 256 + (((4 * s + g) ^ i16) << 4));
        f32x4 o0, o1;
        wgemm(smem + L_WQ, cg, lane, bf, o0, o1);
        const u32x4 qv = {pk_bf16(o0[0], o0[1]), pk_bf16(o0[2], o0[3]), pk_bf16(o1[0], o1[1]), pk_bf16(o1[2], o1[3])};
        *(LAS u32x4*)(smem + L_Q + (nt * 16 + i16) * 256 + (4 * cg + g) * 16) = qv;
    }
    ST(6);
    const u32 m0 = (lane & 3) == 0 ? 0x0000ffffu : (lane & 3) == 1 ? 0xffff0000u : 0u;
    const u32 m1 = (lane & 3) == 2 ? 0x0000ffffu : (lane & 3) == 3 ? 0xffff0000u : 0u;
    LAS u32* cnts = (LAS u32*)(smem + L_CNT);
    LAS u32* list = (LAS u32*)(smem + L_LIST);
    f32x4 tot0 = {0.f, 0.f, 0.f, 0.f}, tot1 = tot0;
    for (int done = 0;; done += 32) {
        const int rem = cnt - done - kk * 4;
        u32 ra[4], rb[4];
#pragma unroll
        for (int j = 0; j < 4; ++j) ra[j] = (j < rem) ? __hip_atomic_fetch_add(&cnts[ea[j] & 63], 1u, __ATOMIC_RELAXED, __HIP_MEMORY_SCOPE_WORKGROUP) : 0u;
        const bool more = rem > 16;
        if (more) {
#pragma unroll
            for (int j = 0; j < 4; ++j) rb[j] = (j + 16 < rem) ? __hip_atomic_fetch_add(&cnts[eb[j] & 63], 1u, __ATOMIC_RELAXED, __HIP_MEMORY_SCOPE_WORKGROUP) : 0u;
            if (cnt - done > 32) cnts[64] = 1;
        }
        __syncthreads();
        ST(7);
        const u32 c = cnts[lane];
        const int again = __builtin_amdgcn_readfirstlane(cnts[64]);
        const u32 nq = (c + 3) >> 2;
        const u32 inc = dpp_scan_incl(nq);
        const u32 s0 = (inc - nq) * 4;
        const int Tq = __builtin_amdgcn_readlane(inc, 63);
        u32 sj[4];
#pragma unroll
        for (int j = 0; j < 4; ++j) sj[j] = __builtin_amdgcn_ds_bpermute((ea[j] & 63) << 2, s0);
#pragma unroll
        for (int j = 0; j < 4; ++j) if (j < rem) list[sj[j] + ra[j]] = ea[j];
        if (__any(more)) {
#pragma unroll
            for (int j = 0; j < 4; ++j) sj[j] = __builtin_amdgcn_ds_bpermute((eb[j] & 63) << 2, s0);
#pragma unroll
            for (int j = 0; j < 4; ++j) if (j + 16 < rem) list[sj[j] + rb[j]] = eb[j];
        }
        const int n = (Tq + 63) >> 6;
        if (w == 0) {
#pragma unroll
            for (int j = 0; j < 3; ++j) if (c + j < nq * 4) list[s0 + c + j] = DUMMY;
        }
        if (w == 1) { for (int o = lane; o < 16 * n; o += 64) list[Tq * 4 + o] = DUMMY; }
        const u32 mg = n > 0 ? ((1u << 20) + n - 1) / n : 0u;
        const u32 myinc = __builtin_amdgcn_ds_bpermute((4 * w + g) << 2, inc), mynq = __builtin_amdgcn_ds_bpermute((4 * w + g) << 2, nq);
        __syncthreads();
        ST(8);
        if (4 * w * n < Tq) {
            const int gi = 4 * w + g;
            const LAS u32x4* lq = (const LAS u32x4*)list + gi * n;
            f32x4 acc0 = {0.f, 0.f, 0.f, 0.f}, acc1 = acc0;
            const u32 loff = i16 * 16;
            auto fetch = [&](Batch& B, const u32x4 e) {
                B.x0 = __builtin_amdgcn_raw_buffer_load_b128(rx, (e[0] & 0xffffff00u) | loff, 0, 0);
                B.x1 = __builtin_amdgcn_raw_buffer_load_b128(rx, (e[1] & 0xffffff00u) | loff, 0, 0);
                B.x2 = __builtin_amdgcn_raw_buffer_load_b128(rx, (e[2] & 0xffffff00u) | loff, 0, 0);
                B.x3 = __builtin_amdgcn_raw_buffer_load_b128(rx, (e[3] & 0xffffff00u) | loff, 0, 0);
                B.node = e[0] & 63;
                B.q = *(const LAS u32x4*)(smem + L_Q + B.node * 256 + loff);
            };
            u32 cur;
            LAS char* sl = smem + L_SLOT + gi * 512 + loff;
            auto flush = [&]() {
                *(LAS f32x4*)(sl + cur * 512) = acc0;
                *(LAS f32x4*)(sl + cur * 512 + 256) = acc1;
            };
            auto step = [&](const Batch& B) {
                if (B.node != cur) { flush(); acc0 = (f32x4){0.f, 0.f, 0.f, 0.f}; acc1 = acc0; cur = B.node; }
                compute(B, m0, m1, acc0, acc1);
            };
            Batch A, B;
            fetch(A, lq[0]);
            cur = A.node;
            u32x4 en = lq[1];
            int t = 1;
            for (; t + 1 < n; t += 2) {
                fetch(B, en); en = lq[t + 1]; step(A);
                fetch(A, en); en = lq[t + 2]; step(B);
            }
            if (t < n) { fetch(B, en); step(A); step(B); } else step(A);
            flush();
        }
        ST(9);
        __syncthreads();
        ST(10);
        if (mynq > 0) {
            const u32 gf = ((myinc - mynq) * mg) >> 20, gl = ((myinc - 1) * mg) >> 20;
            const LAS char* r = smem + L_SLOT + (4 * w + g) * 512 + i16 * 16;
            for (u32 gg = gf; gg <= gl; ++gg) {
                tot0 += *(const LAS f32x4*)(r + gg * 512);
                tot1 += *(const LAS f32x4*)(r + gg * 512 + 256);
            }
        }
        if (!again) break;
        if (tid < 65) cnts[tid] = 0;
        ea = __builtin_amdgcn_raw_buffer_load_b128(re, ebase + (done + 32) * 4, 0, 0);
        eb = __builtin_amdgcn_raw_buffer_load_b128(re, ebase + (done + 48) * 4, 0, 0);
        __syncthreads();
    }
    {
        const u32x4 v = {pk_bf16(tot0[0], tot0[1]), pk_bf16(tot0[2], tot0[3]), pk_bf16(tot1[0], tot1[1]), pk_bf16(tot1[2], tot1[3])};
        *(LAS u32x4*)(smem + L_Q + lnode * 256 + ((i16 ^ (lnode & 15)) << 4)) = v;
    }
    __syncthreads();
    {
        bf16x8 bf[4];
#pragma unroll
        for (int s = 0; s < 4; ++s)
            bf[s] = *(const LAS bf16x8*)(smem + L_Q + (nt * 16 + i16) * 256 + (((4 * s + g) ^ i16) << 4));
        f32x4 o0, o1;
        wgemm(smem + L_WV, cg, lane, bf, o0, o1);
        const u32x4 ov = {pk_bf16(o0[0], o0[1]), pk_bf16(o0[2], o0[3]), pk_bf16(o1[0], o1[1]), pk_bf16(o1[2], o1[3])};
        const int node = (nt * 16 + i16) * 256 + b;
        if (node < N) *(u32x4*)(out + (size_t)node * 256 + (4 * cg + g) * 16) = ov;
    }
    ST(11);
    ST_END(4, 11);
}

extern "C" void kernel_launch(void* const* d_in, const int* in_sizes, int n_in,
                              void* d_out, int out_size, void* d_ws, size_t ws_size,
                              hipStream_t stream) {
    const char* x = (const char*)d_in[0];
    const int* src = (const int*)d_in[1];
    const int* dst = (const int*)d_in[2];
    const char* wq = (const char*)d_in[3];
    const char* wv = (const char*)d_in[4];
    const int N = in_sizes[0] / 128, E = in_sizes[1];
    const int CH = ((E + NB * 4 - 1) / (NB * 4)) * 4;
    u32* offs = (u32*)d_ws;
    u32* ent = offs + NB * 256;
    k_bin<<<NB, 1024, 0, stream>>>(src, dst, x, ent, offs, E, CH, N * 256);
    k_main<<<NB, 1024, 0, stream>>>(x, wq, wv, ent, offs, (char*)d_out, N, CH);
}
```

```cpp
#include <hip/hip_runtime.h>
#include <stdint.h>

typedef uint32_t u32;
typedef __attribute__((ext_vector_type(4))) uint32_t u32x4;
typedef __attribute__((ext_vector_type(2))) uint32_t u32x2;
typedef __attribute__((ext_vector_type(4))) float f32x4;
typedef __attribute__((ext_vector_type(4))) short s16x4;
typedef __attribute__((ext_vector_type(8))) __bf16 bf16x8;
typedef __attribute__((ext_vector_type(2))) __bf16 bf16x2;
#define LAS __attribute__((address_space(3)))

#define ST_DECL
#define ST(i)
#define ST_END(lo, hi)
#define CODEPF 0
#define XPF 0

constexpr int CODE_LPW = 4;
constexpr int NB = 256;
constexpr u32 EMASK = 0x003FFF00u;
constexpr u32 NONE = EMASK | 63u;
constexpr u32 TAG0 = 0xFFC00000u, TAG = 0xFFC000C0u, F_MORE = 0x40u, F_OVER = 0x80u;
constexpr int WS_A = 4096, WS_B = WS_A + NB * NB * 64, WS_END = WS_B + NB * NB * 64;

constexpr int LIST_CAP = 8192 + 768;
constexpr int STG_S = 144;
constexpr int L_WV = 0, L_Q = 32768, L_SLOT = L_Q + 16384, L_STG = L_SLOT, L_XS = L_SLOT + 256 * STG_S, L_LIST = L_SLOT + 128 * 512, L_WQ = L_LIST;
constexpr int L_HIST = L_LIST + LIST_CAP * 4, L_CNT = L_HIST + 1024, L_OVF = L_CNT + 256, L_END = L_OVF + 32;

static __device__ __forceinline__ u32 dpp_scan_incl(u32 v) {
    v += __builtin_amdgcn_update_dpp(0u, v, 0x111, 0xf, 0xf, false);
    v += __builtin_amdgcn_update_dpp(0u, v, 0x112, 0xf, 0xf, false);
    v += __builtin_amdgcn_update_dpp(0u, v, 0x114, 0xf, 0xf, false);
    v += __builtin_amdgcn_update_dpp(0u, v, 0x118, 0xf, 0xf, false);
    v += __builtin_amdgcn_update_dpp(0u, v, 0x142, 0xa, 0xf, false);
    v += __builtin_amdgcn_update_dpp(0u, v, 0x143, 0xc, 0xf, false);
    return v;
}
static __device__ __forceinline__ u32 pk_bf16(float a, float b) {
    bf16x2 v = {(__bf16)a, (__bf16)b};
    return __builtin_bit_cast(u32, v);
}
static __device__ __forceinline__ u32 lds_add(LAS u32* p, u32 v) { return __hip_atomic_fetch_add(p, v, __ATOMIC_RELAXED, __HIP_MEMORY_SCOPE_WORKGROUP); }
static __device__ __forceinline__ f32x4 mfma444(u32x2 a, u32x2 b, f32x4 c) {
    return __builtin_amdgcn_mfma_f32_4x4x4bf16_1k(__builtin_bit_cast(s16x4, a), __builtin_bit_cast(s16x4, b), c, 0, 0, 0);
}
template <int CTRL> static __device__ __forceinline__ float dppf(float v) {
    return __builtin_bit_cast(float, __builtin_amdgcn_update_dpp(0u, __builtin_bit_cast(u32, v), CTRL, 0xf, 0xf, true));
}
template <int CTRL> static __device__ __forceinline__ u32 dppu(u32 v) {
    return __builtin_amdgcn_update_dpp(0u, v, CTRL, 0xf, 0xf, true);
}
static __device__ __forceinline__ float diag4(f32x4 d) {
    float t = d[0] + dppf<0x55>(d[1]);
    t += dppf<0xAA>(d[2]);
    t += dppf<0xFF>(d[3]);
    return t;
}
struct Batch { u32x4 x0, x1, x2, x3, q; u32 node; };

static __device__ __forceinline__ void compute(const Batch& b, u32 m0, u32 m1, f32x4& acc0, f32x4& acc1) {
    const f32x4 z = {0.f, 0.f, 0.f, 0.f};
    const u32x2 ql = b.q.xy, qh = b.q.zw;
    f32x4 d0 = mfma444(b.x0.xy, ql, z), d1 = mfma444(b.x1.xy, ql, z), d2 = mfma444(b.x2.xy, ql, z), d3 = mfma444(b.x3.xy, ql, z);
    d0 = mfma444(b.x0.zw, qh, d0); d1 = mfma444(b.x1.zw, qh, d1); d2 = mfma444(b.x2.zw, qh, d2); d3 = mfma444(b.x3.zw, qh, d3);
    const float a = diag4(d0), bb = diag4(d1), c = diag4(d2), d = diag4(d3);
    float t0, t1, u;
    asm("s_nop 1\n\t"
        "v_add_f32_dpp %0, %3, %3 row_ror:8 row_mask:0xf bank_mask:0x3\n\t"
        "v_add_f32_dpp %1, %5, %5 row_ror:8 row_mask:0xf bank_mask:0x3\n\t"
        "v_add_f32_dpp %0, %4, %4 row_ror:8 row_mask:0xf bank_mask:0xc\n\t"
        "v_add_f32_dpp %1, %6, %6 row_ror:8 row_mask:0xf bank_mask:0xc\n\t"
        "s_nop 0\n\t"
        "v_add_f32_dpp %2, %0, %0 row_ror:12 row_mask:0xf bank_mask:0x5\n\t"
        "v_add_f32_dpp %2, %1, %1 row_ror:4 row_mask:0xf bank_mask:0xa"
        : "=&v"(t0), "=&v"(t1), "=&v"(u) : "v"(a), "v"(bb), "v"(c), "v"(d));
    const float al = __builtin_amdgcn_rcpf(1.0f + __builtin_amdgcn_exp2f(u));
    const u32 a2 = pk_bf16(al, al);
    { const u32x2 A = {dppu<0x150>(a2) & m0, dppu<0x150>(a2) & m1}; acc0 = mfma444(A, b.x0.xy, acc0); acc1 = mfma444(A, b.x0.zw, acc1); }
    { const u32x2 A = {dppu<0x158>(a2) & m0, dppu<0x158>(a2) & m1}; acc0 = mfma444(A, b.x1.xy, acc0); acc1 = mfma444(A, b.x1.zw, acc1); }
    { const u32x2 A = {dppu<0x154>(a2) & m0, dppu<0x154>(a2) & m1}; acc0 = mfma444(A, b.x2.xy, acc0); acc1 = mfma444(A, b.x2.zw, acc1); }
    { const u32x2 A = {dppu<0x15C>(a2) & m0, dppu<0x15C>(a2) & m1}; acc0 = mfma444(A, b.x3.xy, acc0); acc1 = mfma444(A, b.x3.zw, acc1); }
}

static __device__ __forceinline__ void wgemm(const LAS char* wimg, const LAS char* rows, int nt, int cg, int lane, f32x4& o0, f32x4& o1) {
    const int kg = lane >> 4, q = (lane >> 2) & 3, p = lane & 3, i16 = lane & 15;
    const int swz = ((q >> 1) << 2) | ((kg & 1) << 3);
    const int base = (8 * kg + q) * 256 + (((4 * cg + p) ^ swz) << 4);
    const int h0 = (q & 1) << 3, h1 = h0 ^ 8;
    o0 = (f32x4){0.f, 0.f, 0.f, 0.f}; o1 = o0;
#pragma unroll
    for (int s = 0; s < 4; ++s) {
        const bf16x8 bf = *(const LAS bf16x8*)(rows + (nt * 16 + i16) * 256 + (((4 * s + kg) ^ i16) << 4));
        const s16x4 a00 = __builtin_amdgcn_ds_read_tr16_b64_v4i16((LAS s16x4*)(wimg + base + h0 + s * 8192));
        const s16x4 a01 = __builtin_amdgcn_ds_read_tr16_b64_v4i16((LAS s16x4*)(wimg + base + h0 + s * 8192 + 1024));
        const s16x4 a10 = __builtin_amdgcn_ds_read_tr16_b64_v4i16((LAS s16x4*)(wimg + base + h1 + s * 8192));
        const s16x4 a11 = __builtin_amdgcn_ds_read_tr16_b64_v4i16((LAS s16x4*)(wimg + base + h1 + s * 8192 + 1024));
        const bf16x8 A0 = __builtin_bit_cast(bf16x8, __builtin_shufflevector(a00, a01, 0, 1, 2, 3, 4, 5, 6, 7));
        const bf16x8 A1 = __builtin_bit_cast(bf16x8, __builtin_shufflevector(a10, a11, 0, 1, 2, 3, 4, 5, 6, 7));
        o0 = __builtin_amdgcn_mfma_f32_16x16x32_bf16(A0, bf, o0, 0, 0, 0);
        o1 = __builtin_amdgcn_mfma_f32_16x16x32_bf16(A1, bf, o1, 0, 0, 0);
    }
}

static __device__ __forceinline__ u32x4 poll_piece(__amdgpu_buffer_rsrc_t rw, int off, u32x4 tg) {
    u32x4 v;
    for (int spin = 0; spin < (1 << 20); ++spin) {
        asm volatile("" ::: "memory");
        v = __builtin_amdgcn_raw_buffer_load_b128(rw, off, 0, 16);
        const u32 bad = ((v[0] ^ tg[0]) & TAG0) | (((v[1] ^ tg[1]) | (v[2] ^ tg[2]) | (v[3] ^ tg[3])) & TAG);
        if (bad == 0) break;
    }
    return v;
}

__global__ __launch_bounds__(1024) void k_graph(const char* __restrict__ x, const int* __restrict__ src, const int* __restrict__ dst,
                                                const char* __restrict__ wq, const char* __restrict__ wv,
                                                char* __restrict__ out, char* ws, int N, int E
) {
    __shared__ __attribute__((aligned(16))) char smem_[L_END];
    LAS char* smem = (LAS char*)smem_;
    const int tid = threadIdx.x, lane = tid & 63;
    const int w = __builtin_amdgcn_readfirstlane(tid >> 6);
    const int b = blockIdx.x;
    ST_DECL;
    ST(0);
    u32 pf = 0;
    asm volatile("" :: "s"(x), "s"(src), "s"(dst), "s"(wq), "s"(wv), "s"(out), "s"(ws), "s"(N), "s"(E));
    const int CH = ((E + NB * 4 - 1) / (NB * 4)) * 4;
    const auto rx = __builtin_amdgcn_make_buffer_rsrc((void*)x, 0, N * 256, 0x00020000);
    const auto rd = __builtin_amdgcn_make_buffer_rsrc((void*)dst, 0, E * 4, 0x00020000);
    const auto rs = __builtin_amdgcn_make_buffer_rsrc((void*)src, 0, E * 4, 0x00020000);
    const auto rw = __builtin_amdgcn_make_buffer_rsrc((void*)ws, 0, WS_END, 0x00020000);
    const int g = lane >> 4, i16 = lane & 15;
    LAS u32* hist = (LAS u32*)(smem + L_HIST);
    LAS u32* cnts = (LAS u32*)(smem + L_CNT);
    LAS u32* ovf = (LAS u32*)(smem + L_OVF);
    LAS u32* list = (LAS u32*)(smem + L_LIST);

    u32x4 tg;
    {
        const u32x4 o = *(const __attribute__((address_space(4))) u32x4*)ws;
        const u32 c0 = (((o[0] >> 22) << 12) | ((o[1] >> 22) << 2) | ((o[1] >> 6) & 3)) + 1;
        const u32 c1 = (((o[2] >> 22) << 14) | (((o[2] >> 6) & 3) << 12) | ((o[3] >> 22) << 2) | ((o[3] >> 6) & 3)) + 1;
        tg[0] = (c0 >> 12) << 22;
        tg[1] = ((c0 >> 2) << 22) | ((c0 & 3) << 6);
        tg[2] = ((c1 >> 14) << 22) | (((c1 >> 12) & 3) << 6);
        tg[3] = ((c1 >> 2) << 22) | ((c1 & 3) << 6);
    }
    {
        const u32x4 none = {NONE, NONE, NONE, NONE};
        auto init = [&]() {
            LAS char* r = smem + L_STG + (tid >> 2) * STG_S + (tid & 3) * 16;
            *(LAS u32x4*)r = none; *(LAS u32x4*)(r + 64) = none;
            if (tid < 256 + 64 + 8) hist[tid] = 0;
        };
        if (w < 8) {
            const int e0 = b * CH + tid * 4;
            const bool have = tid * 4 < CH && e0 < E;
            const u32x4 d4 = __builtin_amdgcn_raw_buffer_load_b128(rd, have ? e0 * 4 : -16, 0, 0);
            const u32x4 s4 = __builtin_amdgcn_raw_buffer_load_b128(rs, have ? e0 * 4 : -16, 0, 0);
            init();
            __syncthreads();
            if (have) {
                u32 rank[4];
#pragma unroll
                for (int j = 0; j < 4; ++j) rank[j] = lds_add(&hist[d4[j] & 255], 1u);
#pragma unroll
                for (int j = 0; j < 4; ++j)
                    *(LAS u32*)(smem + L_STG + (d4[j] & 255) * STG_S + min(rank[j], 32u) * 4) = (s4[j] << 8) | (d4[j] >> 8);
            }
        } else {
            const int v = w - 8;
            u32x4 xr[2], wqv[4], wvv[4];
#pragma unroll
            for (int j = 0; j < 4; ++j) wqv[j] = *(const u32x4*)(wq + (16 * v + 4 * g + j) * 256 + i16 * 16);
#pragma unroll
            for (int j = 0; j < 2; ++j) xr[j] = __builtin_amdgcn_raw_buffer_load_b128(rx, ((8 * v + 2 * g + j) * 256 + b) * 256 + i16 * 16, 0, 0);
#pragma unroll
            for (int j = 0; j < 4; ++j) wvv[j] = *(const u32x4*)(wv + (16 * v + 4 * g + j) * 256 + i16 * 16);
            init();
            __syncthreads();
#pragma unroll
            for (int j = 0; j < 4; ++j) {
                const int o = (16 * v + 4 * g + j) * 256 + ((i16 ^ (((j >> 1) << 2) | ((g >> 1) << 3))) << 4);
                *(LAS u32x2*)(smem + L_WQ + o + (j & 1) * 8) = wqv[j].xy; *(LAS u32x2*)(smem + L_WQ + o + 8 - (j & 1) * 8) = wqv[j].zw;
                *(LAS u32x2*)(smem + L_WV + o + (j & 1) * 8) = wvv[j].xy; *(LAS u32x2*)(smem + L_WV + o + 8 - (j & 1) * 8) = wvv[j].zw;
            }
#pragma unroll
            for (int j = 0; j < 2; ++j) {
                const int ln = 8 * v + 2 * g + j;
                *(LAS u32x4*)(smem + L_XS + ln * 256 + ((i16 ^ (ln & 15)) << 4)) = xr[j];
            }
        }
    }
    asm volatile("s_waitcnt vmcnt(0)" : "+v"(pf));
    ST(1);
    __syncthreads();
    ST(2);
    {
        const LAS char* r = smem + L_STG + (tid >> 2) * STG_S + (tid & 3) * 16;
        const u32 c = hist[tid >> 2];
        u32x4 v = *(const LAS u32x4*)r | tg;
        v[0] |= (c > 16 ? F_MORE : 0u) | (c > 32 ? F_OVER : 0u);
        const int off = ((tid >> 2) * NB + b) * 64 + (tid & 3) * 16;
        __builtin_amdgcn_raw_buffer_store_b128(v, rw, WS_A + off, 0, 16);
        if (c > 16) __builtin_amdgcn_raw_buffer_store_b128(*(const LAS u32x4*)(r + 64) | tg, rw, WS_B + off, 0, 16);
    }
    ST(3);
    const int nt = w & 3, cg = w >> 2;
    {
        f32x4 o0, o1;
        wgemm(smem + L_WQ, smem + L_XS, nt, cg, lane, o0, o1);
        const float sc = -0.08838834764831845f * 1.4426950408889634f;
        o0 *= sc; o1 *= sc;
        const u32x4 qv = {pk_bf16(o0[0], o0[1]), pk_bf16(o0[2], o0[3]), pk_bf16(o1[0], o1[1]), pk_bf16(o1[2], o1[3])};
        *(LAS u32x4*)(smem + L_Q + (nt * 16 + i16) * 256 + (4 * cg + g) * 16) = qv;
    }
    ST(4);
    u32x4 ea = poll_piece(rw, WS_A + b * (NB * 64) + tid * 16, tg), eb = {NONE, NONE, NONE, NONE};
    bool more = (ea[0] & (F_MORE | F_OVER)) == F_MORE;
    if (more) eb = poll_piece(rw, WS_B + b * (NB * 64) + tid * 16, tg);
    if (ea[0] & F_OVER) {
        __hip_atomic_fetch_or(&ovf[tid >> 7], 1u << ((tid >> 2) & 31), __ATOMIC_RELAXED, __HIP_MEMORY_SCOPE_WORKGROUP);
        ea = eb;
    }
    ST(5);
    const u32 m0 = (lane & 3) == 0 ? 0x0000ffffu : (lane & 3) == 1 ? 0xffff0000u : 0u;
    const u32 m1 = (lane & 3) == 2 ? 0x0000ffffu : (lane & 3) == 3 ? 0xffff0000u : 0u;
    f32x4 tot0 = {0.f, 0.f, 0.f, 0.f}, tot1 = tot0;
    for (int round = 0;; ++round) {
        u32 ra[4], rb[4];
#pragma unroll
        for (int j = 0; j < 4; ++j) ra[j] = ((ea[j] & 63) != 63) ? lds_add(&cnts[ea[j] & 63], 1u) : 0u;
        if (more) {
#pragma unroll
            for (int j = 0; j < 4; ++j) rb[j] = ((eb[j] & 63) != 63) ? lds_add(&cnts[eb[j] & 63], 1u) : 0u;
        }
        __syncthreads();
        ST(7);
        if (round == 0 && b == 0 && tid == 0) __builtin_amdgcn_raw_buffer_store_b128(tg, rw, 0, 0, 16);
        const u32 c = cnts[lane];
        const u32 ov = lane < 8 ? ovf[lane] : 0u;
        const u32 nq = (c + 3) >> 2;
        const u32 inc = dpp_scan_incl(nq);
        const u32 s0 = (inc - nq) * 4;
        const int Tq = __builtin_amdgcn_readlane(inc, 63);
        u32 sj[4];
#pragma unroll
        for (int j = 0; j < 4; ++j) sj[j] = __builtin_amdgcn_ds_bpermute((ea[j] & 63) << 2, s0);
#pragma unroll
        for (int j = 0; j < 4; ++j) if ((ea[j] & 63) != 63) list[sj[j] + ra[j]] = ea[j];
        if (__any(more)) {
#pragma unroll
            for (int j = 0; j < 4; ++j) sj[j] = __builtin_amdgcn_ds_bpermute((eb[j] & 63) << 2, s0);
#pragma unroll
            for (int j = 0; j < 4; ++j) if ((eb[j] & 63) != 63) list[sj[j] + rb[j]] = eb[j];
        }
        const int n = (Tq + 63) >> 6;
        if (w == 0) {
#pragma unroll
            for (int j = 0; j < 3; ++j) if (c + j < nq * 4) list[s0 + c + j] = NONE;
        }
        if (w == 1) { for (int o = lane; o < 16 * n; o += 64) list[Tq * 4 + o] = NONE; }
        const unsigned long long ovb = __ballot(ov != 0);
        const u32 mg = n > 0 ? ((1u << 20) + n - 1) / n : 0u;
        const u32 myinc = __builtin_amdgcn_ds_bpermute((4 * w + g) << 2, inc), mynq = __builtin_amdgcn_ds_bpermute((4 * w + g) << 2, nq);
        __syncthreads();
        ST(8);
        if (4 * w * n < Tq) {
            const int gi = 4 * w + g;
            const LAS u32x4* lq = (const LAS u32x4*)list + gi * n;
            f32x4 acc0 = {0.f, 0.f, 0.f, 0.f}, acc1 = acc0;
            const u32 loff = i16 * 16;
            auto fetch = [&](Batch& B, const u32x4 e) {
                B.x0 = __builtin_amdgcn_raw_buffer_load_b128(rx, (e[0] & EMASK) | loff, 0, 0);
                B.x1 = __builtin_amdgcn_raw_buffer_load_b128(rx, (e[1] & EMASK) | loff, 0, 0);
                B.x2 = __builtin_amdgcn_raw_buffer_load_b128(rx, (e[2] & EMASK) | loff, 0, 0);
                B.x3 = __builtin_amdgcn_raw_buffer_load_b128(rx, (e[3] & EMASK) | loff, 0, 0);
                B.node = e[0] & 63;
                B.q = *(const LAS u32x4*)(smem + L_Q + B.node * 256 + loff);
            };
            u32 cur;
            LAS char* sl = smem + L_SLOT + gi * 512 + loff;
            auto flush = [&]() {
                *(LAS f32x4*)(sl + cur * 512) = acc0;
                *(LAS f32x4*)(sl + cur * 512 + 256) = acc1;
            };
            auto step = [&](const Batch& B) {
                if (B.node != cur) { flush(); acc0 = (f32x4){0.f, 0.f, 0.f, 0.f}; acc1 = acc0; cur = B.node; }
                compute(B, m0, m1, acc0, acc1);
            };
            Batch A, B;
            fetch(A, lq[0]);
            cur = A.node;
            u32x4 en = lq[1];
            int t = 1;
            for (; t + 1 < n; t += 2) {
                fetch(B, en); en = lq[t + 1]; step(A);
                fetch(A, en); en = lq[t + 2]; step(B);
            }
            if (t < n) { fetch(B, en); step(A); step(B); } else step(A);
            flush();
        }
        ST(9);
        __syncthreads();
        ST(10);
        if (mynq > 0) {
            const u32 gf = ((myinc - mynq) * mg) >> 20, gl = ((myinc - 1) * mg) >> 20;
            const LAS char* r = smem + L_SLOT + (4 * w + g) * 512 + i16 * 16;
            for (u32 gg = gf; gg <= gl; ++gg) {
                tot0 += *(const LAS f32x4*)(r + gg * 512);
                tot1 += *(const LAS f32x4*)(r + gg * 512 + 256);
            }
        }
        if (ovb == 0) break;
        const int wi = __builtin_ctzll(ovb);
        const u32 word = __builtin_amdgcn_readlane(ov, wi);
        const int bi = __builtin_ctz(word);
        const int e0 = (wi * 32 + bi) * CH + tid * 4;
        const bool have = tid * 4 < CH && e0 < E;
        const u32x4 d4 = __builtin_amdgcn_raw_buffer_load_b128(rd, e0 * 4, 0, 0), s4 = __builtin_amdgcn_raw_buffer_load_b128(rs, e0 * 4, 0, 0);
#pragma unroll
        for (int j = 0; j < 4; ++j) ea[j] = (have && (d4[j] & 255) == (u32)b) ? ((s4[j] << 8) | (d4[j] >> 8)) : NONE;
        more = false;
        eb = (u32x4){NONE, NONE, NONE, NONE};
        if (tid < 64) cnts[tid] = 0;
        if (tid == 0) ovf[wi] = word & (word - 1);
        __syncthreads();
    }
    int lane2 = lane;
    asm volatile("" : "+v"(lane2));
    {
        const int g = lane2 >> 4, i16 = lane2 & 15;
        const int ln = 4 * w + g;
        const u32x4 v = {pk_bf16(tot0[0], tot0[1]), pk_bf16(tot0[2], tot0[3]), pk_bf16(tot1[0], tot1[1]), pk_bf16(tot1[2], tot1[3])};
        *(LAS u32x4*)(smem + L_Q + ln * 256 + ((i16 ^ (ln & 15)) << 4)) = v;
    }
    __syncthreads();
    {
        f32x4 o0, o1;
        const int g = lane2 >> 4, i16 = lane2 & 15;
        wgemm(smem + L_WV, smem + L_Q, nt, cg, lane2, o0, o1);
        const u32x4 ov = {pk_bf16(o0[0], o0[1]), pk_bf16(o0[2], o0[3]), pk_bf16(o1[0], o1[1]), pk_bf16(o1[2], o1[3])};
        const int node = (nt * 16 + i16) * 256 + b;
        if (node < N) *(u32x4*)(out + (size_t)node * 256 + (4 * cg + g) * 16) = ov;
    }
    ST(11);
    ST_END(0, 11);
}

extern "C" void kernel_launch(void* const* d_in, const int* in_sizes, int n_in,
                              void* d_out, int out_size, void* d_ws, size_t ws_size,
                              hipStream_t stream) {
    const int N = in_sizes[0] / 128, E = in_sizes[1];
    k_graph<<<NB, 1024, 0, stream>>>((const char*)d_in[0], (const int*)d_in[1], (const int*)d_in[2], (const char*)d_in[3], (const char*)d_in[4],
                                     (char*)d_out, (char*)d_ws, N, E
    );
}
```

```cpp
#define PROBE_A 5
#define PROBE_B 8
#define PROBE_WG 0
#include <hip/hip_runtime.h>
#include <stdint.h>

typedef uint32_t u32;
typedef __attribute__((ext_vector_type(4))) uint32_t u32x4;
typedef __attribute__((ext_vector_type(2))) uint32_t u32x2;
typedef __attribute__((ext_vector_type(4))) float f32x4;
typedef __attribute__((ext_vector_type(4))) short s16x4;
typedef __attribute__((ext_vector_type(8))) __bf16 bf16x8;
typedef __attribute__((ext_vector_type(2))) __bf16 bf16x2;
#define LAS __attribute__((address_space(3)))

#define ST_DECL unsigned long long pa_ = 0, pb_ = 0
#define PCLK() wall_clock64()
#define ST(i) do { if ((i) == PROBE_A) pa_ = PCLK(); if ((i) == PROBE_B) pb_ = PCLK(); } while (0)
#define ST_END(lo, hi) do { if (blockIdx.x == PROBE_WG && threadIdx.x == 0) { const unsigned long long t_ = wall_clock64(); while (wall_clock64() - t_ < 8 * (pb_ - pa_)) __builtin_amdgcn_s_sleep(1); } } while (0)
#define CODEPF 1
#define XPF 0

constexpr int CODE_LPW = 4;
constexpr int NB = 256;
constexpr u32 EMASK = 0x003FFF00u;
constexpr u32 NONE = EMASK | 63u;
constexpr u32 TAG0 = 0xFFC00000u, TAG = 0xFFC000C0u, F_MORE = 0x40u, F_OVER = 0x80u;
constexpr int WS_A = 4096, WS_B = WS_A + NB * NB * 64, WS_END = WS_B + NB * NB * 64;

constexpr int LIST_CAP = 8192 + 768;
constexpr int STG_S = 144;
constexpr int L_WV = 0, L_Q = 32768, L_SLOT = L_Q + 16384, L_STG = L_SLOT, L_XS = L_SLOT + 256 * STG_S, L_LIST = L_SLOT + 128 * 512, L_WQ = L_LIST;
constexpr int L_HIST = L_LIST + LIST_CAP * 4, L_CNT = L_HIST + 1024, L_OVF = L_CNT + 256, L_END = L_OVF + 32;

static __device__ __forceinline__ u32 dpp_scan_incl(u32 v) {
    v += __builtin_amdgcn_update_dpp(0u, v, 0x111, 0xf, 0xf, false);
    v += __builtin_amdgcn_update_dpp(0u, v, 0x112, 0xf, 0xf, false);
    v += __builtin_amdgcn_update_dpp(0u, v, 0x114, 0xf, 0xf, false);
    v += __builtin_amdgcn_update_dpp(0u, v, 0x118, 0xf, 0xf, false);
    v += __builtin_amdgcn_update_dpp(0u, v, 0x142, 0xa, 0xf, false);
    v += __builtin_amdgcn_update_dpp(0u, v, 0x143, 0xc, 0xf, false);
    return v;
}
static __device__ __forceinline__ u32 pk_bf16(float a, float b) {
    bf16x2 v = {(__bf16)a, (__bf16)b};
    return __builtin_bit_cast(u32, v);
}
static __device__ __forceinline__ u32 lds_add(LAS u32* p, u32 v) { return __hip_atomic_fetch_add(p, v, __ATOMIC_RELAXED, __HIP_MEMORY_SCOPE_WORKGROUP); }
static __device__ __forceinline__ f32x4 mfma444(u32x2 a, u32x2 b, f32x4 c) {
    return __builtin_amdgcn_mfma_f32_4x4x4bf16_1k(__builtin_bit_cast(s16x4, a), __builtin_bit_cast(s16x4, b), c, 0, 0, 0);
}
template <int CTRL> static __device__ __forceinline__ float dppf(float v) {
    return __builtin_bit_cast(float, __builtin_amdgcn_update_dpp(0u, __builtin_bit_cast(u32, v), CTRL, 0xf, 0xf, true));
}
template <int CTRL> static __device__ __forceinline__ u32 dppu(u32 v) {
    return __builtin_amdgcn_update_dpp(0u, v, CTRL, 0xf, 0xf, true);
}
static __device__ __forceinline__ float diag4(f32x4 d) {
    float t = d[0] + dppf<0x55>(d[1]);
    t += dppf<0xAA>(d[2]);
    t += dppf<0xFF>(d[3]);
    return t;
}
struct Batch { u32x4 x0, x1, x2, x3, q; u32 node; };

static __device__ __forceinline__ void compute(const Batch& b, u32 m0, u32 m1, f32x4& acc0, f32x4& acc1) {
    const f32x4 z = {0.f, 0.f, 0.f, 0.f};
    const u32x2 ql = b.q.xy, qh = b.q.zw;
    f32x4 d0 = mfma444(b.x0.xy, ql, z), d1 = mfma444(b.x1.xy, ql, z), d2 = mfma444(b.x2.xy, ql, z), d3 = mfma444(b.x3.xy, ql, z);
    d0 = mfma444(b.x0.zw, qh, d0); d1 = mfma444(b.x1.zw, qh, d1); d2 = mfma444(b.x2.zw, qh, d2); d3 = mfma444(b.x3.zw, qh, d3);
    const float a = diag4(d0), bb = diag4(d1), c = diag4(d2), d = diag4(d3);
    float t0, t1, u;
    asm("s_nop 1\n\t"
        "v_add_f32_dpp %0, %3, %3 row_ror:8 row_mask:0xf bank_mask:0x3\n\t"
        "v_add_f32_dpp %1, %5, %5 row_ror:8 row_mask:0xf bank_mask:0x3\n\t"
        "v_add_f32_dpp %0, %4, %4 row_ror:8 row_mask:0xf bank_mask:0xc\n\t"
        "v_add_f32_dpp %1, %6, %6 row_ror:8 row_mask:0xf bank_mask:0xc\n\t"
        "s_nop 0\n\t"
        "v_add_f32_dpp %2, %0, %0 row_ror:12 row_mask:0xf bank_mask:0x5\n\t"
        "v_add_f32_dpp %2, %1, %1 row_ror:4 row_mask:0xf bank_mask:0xa"
        : "=&v"(t0), "=&v"(t1), "=&v"(u) : "v"(a), "v"(bb), "v"(c), "v"(d));
    const float al = __builtin_amdgcn_rcpf(1.0f + __builtin_amdgcn_exp2f(u));
    const u32 a2 = pk_bf16(al, al);
    { const u32x2 A = {dppu<0x150>(a2) & m0, dppu<0x150>(a2) & m1}; acc0 = mfma444(A, b.x0.xy, acc0); acc1 = mfma444(A, b.x0.zw, acc1); }
    { const u32x2 A = {dppu<0x158>(a2) & m0, dppu<0x158>(a2) & m1}; acc0 = mfma444(A, b.x1.xy, acc0); acc1 = mfma444(A, b.x1.zw, acc1); }
    { const u32x2 A = {dppu<0x154>(a2) & m0, dppu<0x154>(a2) & m1}; acc0 = mfma444(A, b.x2.xy, acc0); acc1 = mfma444(A, b.x2.zw, acc1); }
    { const u32x2 A = {dppu<0x15C>(a2) & m0, dppu<0x15C>(a2) & m1}; acc0 = mfma444(A, b.x3.xy, acc0); acc1 = mfma444(A, b.x3.zw, acc1); }
}

static __device__ __forceinline__ void wgemm(const LAS char* wimg, const LAS char* rows, int nt, int cg, int lane, f32x4& o0, f32x4& o1) {
    const int kg = lane >> 4, q = (lane >> 2) & 3, p = lane & 3, i16 = lane & 15;
    const int swz = ((q >> 1) << 2) | ((kg & 1) << 3);
    const int base = (8 * kg + q) * 256 + (((4 * cg + p) ^ swz) << 4);
    const int h0 = (q & 1) << 3, h1 = h0 ^ 8;
    o0 = (f32x4){0.f, 0.f, 0.f, 0.f}; o1 = o0;
#pragma unroll
    for (int s = 0; s < 4; ++s) {
        const bf16x8 bf = *(const LAS bf16x8*)(rows + (nt * 16 + i16) * 256 + (((4 * s + kg) ^ i16) << 4));
        const s16x4 a00 = __builtin_amdgcn_ds_read_tr16_b64_v4i16((LAS s16x4*)(wimg + base + h0 + s * 8192));
        const s16x4 a01 = __builtin_amdgcn_ds_read_tr16_b64_v4i16((LAS s16x4*)(wimg + base + h0 + s * 8192 + 1024));
        const s16x4 a10 = __builtin_amdgcn_ds_read_tr16_b64_v4i16((LAS s16x4*)(wimg + base + h1 + s * 8192));
        const s16x4 a11 = __builtin_amdgcn_ds_read_tr16_b64_v4i16((LAS s16x4*)(wimg + base + h1 + s * 8192 + 1024));
        const bf16x8 A0 = __builtin_bit_cast(bf16x8, __builtin_shufflevector(a00, a01, 0, 1, 2, 3, 4, 5, 6, 7));
        const bf16x8 A1 = __builtin_bit_cast(bf16x8, __builtin_shufflevector(a10, a11, 0, 1, 2, 3, 4, 5, 6, 7));
        o0 = __builtin_amdgcn_mfma_f32_16x16x32_bf16(A0, bf, o0, 0, 0, 0);
        o1 = __builtin_amdgcn_mfma_f32_16x16x32_bf16(A1, bf, o1, 0, 0, 0);
    }
}

static __device__ __forceinline__ u32x4 poll_piece(__amdgpu_buffer_rsrc_t rw, int off, u32x4 tg) {
    u32x4 v;
    for (int spin = 0; spin < (1 << 20); ++spin) {
        asm volatile("" ::: "memory");
        v = __builtin_amdgcn_raw_buffer_load_b128(rw, off, 0, 16);
        const u32 bad = ((v[0] ^ tg[0]) & TAG0) | (((v[1] ^ tg[1]) | (v[2] ^ tg[2]) | (v[3] ^ tg[3])) & TAG);
        if (bad == 0) break;
    }
    return v;
}

__global__ __launch_bounds__(1024) void k_graph(const char* __restrict__ x, const int* __restrict__ src, const int* __restrict__ dst,
                                                const char* __restrict__ wq, const char* __restrict__ wv,
                                                char* __restrict__ out, char* ws, int N, int E
) {
    __shared__ __attribute__((aligned(16))) char smem_[L_END];
    LAS char* smem = (LAS char*)smem_;
    const int tid = threadIdx.x, lane = tid & 63;
    const int w = __builtin_amdgcn_readfirstlane(tid >> 6);
    const int b = blockIdx.x;
    ST_DECL;
    ST(0);
    u32 pf = 0;
    {
        uint64_t pc; asm volatile("s_getpc_b64 %0" : "=s"(pc));
        const u32 off = (w * CODE_LPW + min(lane, CODE_LPW - 1)) * 128;
        asm volatile("global_load_dword %0, %1, %2" : "=v"(pf) : "v"(off), "s"(pc));
    }
    asm volatile("" :: "s"(x), "s"(src), "s"(dst), "s"(wq), "s"(wv), "s"(out), "s"(ws), "s"(N), "s"(E));
    const int CH = ((E + NB * 4 - 1) / (NB * 4)) * 4;
    const auto rx = __builtin_amdgcn_make_buffer_rsrc((void*)x, 0, N * 256, 0x00020000);
    const auto rd = __builtin_amdgcn_make_buffer_rsrc((void*)dst, 0, E * 4, 0x00020000);
    const auto rs = __builtin_amdgcn_make_buffer_rsrc((void*)src, 0, E * 4, 0x00020000);
    const auto rw = __builtin_amdgcn_make_buffer_rsrc((void*)ws, 0, WS_END, 0x00020000);
    const int g = lane >> 4, i16 = lane & 15;
    LAS u32* hist = (LAS u32*)(smem + L_HIST);
    LAS u32* cnts = (LAS u32*)(smem + L_CNT);
    LAS u32* ovf = (LAS u32*)(smem + L_OVF);
    LAS u32* list = (LAS u32*)(smem + L_LIST);

    u32x4 tg;
    {
        const u32x4 o = *(const __attribute__((address_space(4))) u32x4*)ws;
        const u32 c0 = (((o[0] >> 22) << 12) | ((o[1] >> 22) << 2) | ((o[1] >> 6) & 3)) + 1;
        const u32 c1 = (((o[2] >> 22) << 14) | (((o[2] >> 6) & 3) << 12) | ((o[3] >> 22) << 2) | ((o[3] >> 6) & 3)) + 1;
        tg[0] = (c0 >> 12) << 22;
        tg[1] = ((c0 >> 2) << 22) | ((c0 & 3) << 6);
        tg[2] = ((c1 >> 14) << 22) | (((c1 >> 12) & 3) << 6);
        tg[3] = ((c1 >> 2) << 22) | ((c1 & 3) << 6);
    }
    {
        const u32x4 none = {NONE, NONE, NONE, NONE};
        auto init = [&]() {
            LAS char* r = smem + L_STG + (tid >> 2) * STG_S + (tid & 3) * 16;
            *(LAS u32x4*)r = none; *(LAS u32x4*)(r + 64) = none;
            if (tid < 256 + 64 + 8) hist[tid] = 0;
        };
        if (w < 8) {
            const int e0 = b * CH + tid * 4;
            const bool have = tid * 4 < CH && e0 < E;
            const u32x4 d4 = __builtin_amdgcn_raw_buffer_load_b128(rd, have ? e0 * 4 : -16, 0, 0);
            const u32x4 s4 = __builtin_amdgcn_raw_buffer_load_b128(rs, have ? e0 * 4 : -16, 0, 0);
            init();
            __syncthreads();
            if (have) {
                u32 rank[4];
#pragma unroll
                for (int j = 0; j < 4; ++j) rank[j] = lds_add(&hist[d4[j] & 255], 1u);
#pragma unroll
                for (int j = 0; j < 4; ++j)
                    *(LAS u32*)(smem + L_STG + (d4[j] & 255) * STG_S + min(rank[j], 32u) * 4) = (s4[j] << 8) | (d4[j] >> 8);
            }
        } else {
            const int v = w - 8;
            u32x4 xr[2], wqv[4], wvv[4];
#pragma unroll
            for (int j = 0; j < 4; ++j) wqv[j] = *(const u32x4*)(wq + (16 * v + 4 * g + j) * 256 + i16 * 16);
#pragma unroll
            for (int j = 0; j < 2; ++j) xr[j] = __builtin_amdgcn_raw_buffer_load_b128(rx, ((8 * v + 2 * g + j) * 256 + b) * 256 + i16 * 16, 0, 0);
#pragma unroll
            for (int j = 0; j < 4; ++j) wvv[j] = *(const u32x4*)(wv + (16 * v + 4 * g + j) * 256 + i16 * 16);
            init();
            __syncthreads();
#pragma unroll
            for (int j = 0; j < 4; ++j) {
                const int o = (16 * v + 4 * g + j) * 256 + ((i16 ^ (((j >> 1) << 2) | ((g >> 1) << 3))) << 4);
                *(LAS u32x2*)(smem + L_WQ + o + (j & 1) * 8) = wqv[j].xy; *(LAS u32x2*)(smem + L_WQ + o + 8 - (j & 1) * 8) = wqv[j].zw;
                *(LAS u32x2*)(smem + L_WV + o + (j & 1) * 8) = wvv[j].xy; *(LAS u32x2*)(smem + L_WV + o + 8 - (j & 1) * 8) = wvv[j].zw;
            }
#pragma unroll
            for (int j = 0; j < 2; ++j) {
                const int ln = 8 * v + 2 * g + j;
                *(LAS u32x4*)(smem + L_XS + ln * 256 + ((i16 ^ (ln & 15)) << 4)) = xr[j];
            }
        }
    }
    asm volatile("s_waitcnt vmcnt(0)" : "+v"(pf));
    ST(1);
    __syncthreads();
    ST(2);
    {
        const LAS char* r = smem + L_STG + (tid >> 2) * STG_S + (tid & 3) * 16;
        const u32 c = hist[tid >> 2];
        u32x4 v = *(const LAS u32x4*)r | tg;
        v[0] |= (c > 16 ? F_MORE : 0u) | (c > 32 ? F_OVER : 0u);
        const int off = ((tid >> 2) * NB + b) * 64 + (tid & 3) * 16;
        __builtin_amdgcn_raw_buffer_store_b128(v, rw, WS_A + off, 0, 16);
        if (c > 16) __builtin_amdgcn_raw_buffer_store_b128(*(const LAS u32x4*)(r + 64) | tg, rw, WS_B + off, 0, 16);
    }
    ST(3);
    const int nt = w & 3, cg = w >> 2;
    {
        f32x4 o0, o1;
        wgemm(smem + L_WQ, smem + L_XS, nt, cg, lane, o0, o1);
        const float sc = -0.08838834764831845f * 1.4426950408889634f;
        o0 *= sc; o1 *= sc;
        const u32x4 qv = {pk_bf16(o0[0], o0[1]), pk_bf16(o0[2], o0[3]), pk_bf16(o1[0], o1[1]), pk_bf16(o1[2], o1[3])};
        *(LAS u32x4*)(smem + L_Q + (nt * 16 + i16) * 256 + (4 * cg + g) * 16) = qv;
    }
    ST(4);
    u32x4 ea = poll_piece(rw, WS_A + b * (NB * 64) + tid * 16, tg), eb = {NONE, NONE, NONE, NONE};
    bool more = (ea[0] & (F_MORE | F_OVER)) == F_MORE;
    if (more) eb = poll_piece(rw, WS_B + b * (NB * 64) + tid * 16, tg);
    if (ea[0] & F_OVER) {
        __hip_atomic_fetch_or(&ovf[tid >> 7], 1u << ((tid >> 2) & 31), __ATOMIC_RELAXED, __HIP_MEMORY_SCOPE_WORKGROUP);
        ea = eb;
    }
    ST(5);
    const u32 m0 = (lane & 3) == 0 ? 0x0000ffffu : (lane & 3) == 1 ? 0xffff0000u : 0u;
    const u32 m1 = (lane & 3) == 2 ? 0x0000ffffu : (lane & 3) == 3 ? 0xffff0000u : 0u;
    f32x4 tot0 = {0.f, 0.f, 0.f, 0.f}, tot1 = tot0;
    for (int round = 0;; ++round) {
        u32 ra[4], rb[4];
#pragma unroll
        for (int j = 0; j < 4; ++j) ra[j] = ((ea[j] & 63) != 63) ? lds_add(&cnts[ea[j] & 63], 1u) : 0u;
        if (more) {
#pragma unroll
            for (int j = 0; j < 4; ++j) rb[j] = ((eb[j] & 63) != 63) ? lds_add(&cnts[eb[j] & 63], 1u) : 0u;
        }
        __syncthreads();
        ST(7);
        if (round == 0 && b == 0 && tid == 0) __builtin_amdgcn_raw_buffer_store_b128(tg, rw, 0, 0, 16);
        const u32 c = cnts[lane];
        const u32 ov = lane < 8 ? ovf[lane] : 0u;
        const u32 nq = (c + 3) >> 2;
        const u32 inc = dpp_scan_incl(nq);
        const u32 s0 = (inc - nq) * 4;
        const int Tq = __builtin_amdgcn_readlane(inc, 63);
        u32 sj[4];
#pragma unroll
        for (int j = 0; j < 4; ++j) sj[j] = __builtin_amdgcn_ds_bpermute((ea[j] & 63) << 2, s0);
#pragma unroll
        for (int j = 0; j < 4; ++j) if ((ea[j] & 63) != 63) list[sj[j] + ra[j]] = ea[j];
        if (__any(more)) {
#pragma unroll
            for (int j = 0; j < 4; ++j) sj[j] = __builtin_amdgcn_ds_bpermute((eb[j] & 63) << 2, s0);
#pragma unroll
            for (int j = 0; j < 4; ++j) if ((eb[j] & 63) != 63) list[sj[j] + rb[j]] = eb[j];
        }
        const int n = (Tq + 63) >> 6;
        if (w == 0) {
#pragma unroll
            for (int j = 0; j < 3; ++j) if (c + j < nq * 4) list[s0 + c + j] = NONE;
        }
        if (w == 1) { for (int o = lane; o < 16 * n; o += 64) list[Tq * 4 + o] = NONE; }
        const unsigned long long ovb = __ballot(ov != 0);
        const u32 mg = n > 0 ? ((1u << 20) + n - 1) / n : 0u;
        const u32 myinc = __builtin_amdgcn_ds_bpermute((4 * w + g) << 2, inc), mynq = __builtin_amdgcn_ds_bpermute((4 * w + g) << 2, nq);
        __syncthreads();
        ST(8);
        if (4 * w * n < Tq) {
            const int gi = 4 * w + g;
            const LAS u32x4* lq = (const LAS u32x4*)list + gi * n;
            f32x4 acc0 = {0.f, 0.f, 0.f, 0.f}, acc1 = acc0;
            const u32 loff = i16 * 16;
            auto fetch = [&](Batch& B, const u32x4 e) {
                B.x0 = __builtin_amdgcn_raw_buffer_load_b128(rx, (e[0] & EMASK) | loff, 0, 0);
                B.x1 = __builtin_amdgcn_raw_buffer_load_b128(rx, (e[1] & EMASK) | loff, 0, 0);
                B.x2 = __builtin_amdgcn_raw_buffer_load_b128(rx, (e[2] & EMASK) | loff, 0, 0);
                B.x3 = __builtin_amdgcn_raw_buffer_load_b128(rx, (e[3] & EMASK) | loff, 0, 0);
                B.node = e[0] & 63;
                B.q = *(const LAS u32x4*)(smem + L_Q + B.node * 256 + loff);
            };
            u32 cur;
            LAS char* sl = smem + L_SLOT + gi * 512 + loff;
            auto flush = [&]() {
                *(LAS f32x4*)(sl + cur * 512) = acc0;
                *(LAS f32x4*)(sl + cur * 512 + 256) = acc1;
            };
            auto step = [&](const Batch& B) {
                if (B.node != cur) { flush(); acc0 = (f32x4){0.f, 0.f, 0.f, 0.f}; acc1 = acc0; cur = B.node; }
                compute(B, m0, m1, acc0, acc1);
            };
            Batch A, B;
            fetch(A, lq[0]);
            cur = A.node;
            u32x4 en = lq[1];
            int t = 1;
            for (; t + 1 < n; t += 2) {
                fetch(B, en); en = lq[t + 1]; step(A);
                fetch(A, en); en = lq[t + 2]; step(B);
            }
            if (t < n) { fetch(B, en); step(A); step(B); } else step(A);
            flush();
        }
        ST(9);
        __syncthreads();
        ST(10);
        if (mynq > 0) {
            const u32 gf = ((myinc - mynq) * mg) >> 20, gl = ((myinc - 1) * mg) >> 20;
            const LAS char* r = smem + L_SLOT + (4 * w + g) * 512 + i16 * 16;
            for (u32 gg = gf; gg <= gl; ++gg) {
                tot0 += *(const LAS f32x4*)(r + gg * 512);
                tot1 += *(const LAS f32x4*)(r + gg * 512 + 256);
            }
        }
        if (ovb == 0) break;
        const int wi = __builtin_ctzll(ovb);
        const u32 word = __builtin_amdgcn_readlane(ov, wi);
        const int bi = __builtin_ctz(word);
        const int e0 = (wi * 32 + bi) * CH + tid * 4;
        const bool have = tid * 4 < CH && e0 < E;
        const u32x4 d4 = __builtin_amdgcn_raw_buffer_load_b128(rd, e0 * 4, 0, 0), s4 = __builtin_amdgcn_raw_buffer_load_b128(rs, e0 * 4, 0, 0);
#pragma unroll
        for (int j = 0; j < 4; ++j) ea[j] = (have && (d4[j] & 255) == (u32)b) ? ((s4[j] << 8) | (d4[j] >> 8)) : NONE;
        more = false;
        eb = (u32x4){NONE, NONE, NONE, NONE};
        if (tid < 64) cnts[tid] = 0;
        if (tid == 0) ovf[wi] = word & (word - 1);
        __syncthreads();
    }
    int lane2 = lane;
    asm volatile("" : "+v"(lane2));
    {
        const int g = lane2 >> 4, i16 = lane2 & 15;
        const int ln = 4 * w + g;
        const u32x4 v = {pk_bf16(tot0[0], tot0[1]), pk_bf16(tot0[2], tot0[3]), pk_bf16(tot1[0], tot1[1]), pk_bf16(tot1[2], tot1[3])};
        *(LAS u32x4*)(smem + L_Q + ln * 256 + ((i16 ^ (ln & 15)) << 4)) = v;
    }
    __syncthreads();
    {
        f32x4 o0, o1;
        const int g = lane2 >> 4, i16 = lane2 & 15;
        wgemm(smem + L_WV, smem + L_Q, nt, cg, lane2, o0, o1);
        const u32x4 ov = {pk_bf16(o0[0], o0[1]), pk_bf16(o0[2], o0[3]), pk_bf16(o1[0], o1[1]), pk_bf16(o1[2], o1[3])};
        const int node = (nt * 16 + i16) * 256 + b;
        if (node < N) *(u32x4*)(out + (size_t)node * 256 + (4 * cg + g) * 16) = ov;
    }
    ST(11);
    ST_END(0, 11);
}

extern "C" void kernel_launch(void* const* d_in, const int* in_sizes, int n_in,
                              void* d_out, int out_size, void* d_ws, size_t ws_size,
                              hipStream_t stream) {
    const int N = in_sizes[0] / 128, E = in_sizes[1];
    k_graph<<<NB, 1024, 0, stream>>>((const char*)d_in[0], (const int*)d_in[1], (const int*)d_in[2], (const char*)d_in[3], (const char*)d_in[4],
                                     (char*)d_out, (char*)d_ws, N, E
    );
}
```

```cpp
#include <hip/hip_runtime.h>
#include <stdint.h>

typedef uint32_t u32;
typedef __attribute__((ext_vector_type(4))) uint32_t u32x4;
typedef __attribute__((ext_vector_type(2))) uint32_t u32x2;
typedef __attribute__((ext_vector_type(4))) float f32x4;
typedef __attribute__((ext_vector_type(4))) short s16x4;
typedef __attribute__((ext_vector_type(8))) __bf16 bf16x8;
typedef __attribute__((ext_vector_type(2))) __bf16 bf16x2;
#define LAS __attribute__((address_space(3)))

#define ST_DECL
#define ST(i)
#define ST_END(lo, hi)
#define CODEPF 1
#define XPF 0

constexpr int CODE_LPW = 4;
constexpr int NB = 256;
constexpr u32 EMASK = 0x003FFF00u;
constexpr u32 NONE = EMASK | 63u;
constexpr u32 TAG0 = 0xFFC00000u, TAG = 0xFFC000C0u, F_MORE = 0x40u, F_OVER = 0x80u;
constexpr int WS_A = 4096, WS_B = WS_A + NB * NB * 64, WS_END = WS_B + NB * NB * 64;

constexpr int LIST_CAP = 8192 + 768;
constexpr int STG_S = 144;
constexpr int L_WV = 0, L_Q = 32768, L_SLOT = L_Q + 16384, L_STG = L_SLOT, L_XS = L_SLOT + 256 * STG_S, L_LIST = L_SLOT + 128 * 512, L_WQ = L_LIST;
constexpr int L_HIST = L_LIST + LIST_CAP * 4, L_CNT = L_HIST + 1024, L_OVF = L_CNT + 256, L_END = L_OVF + 32;

static __device__ __forceinline__ u32 dpp_scan_incl(u32 v) {
    v += __builtin_amdgcn_update_dpp(0u, v, 0x111, 0xf, 0xf, false);
    v += __builtin_amdgcn_update_dpp(0u, v, 0x112, 0xf, 0xf, false);
    v += __builtin_amdgcn_update_dpp(0u, v, 0x114, 0xf, 0xf, false);
    v += __builtin_amdgcn_update_dpp(0u, v, 0x118, 0xf, 0xf, false);
    v += __builtin_amdgcn_update_dpp(0u, v, 0x142, 0xa, 0xf, false);
    v += __builtin_amdgcn_update_dpp(0u, v, 0x143, 0xc, 0xf, false);
    return v;
}
static __device__ __forceinline__ u32 pk_bf16(float a, float b) {
    bf16x2 v = {(__bf16)a, (__bf16)b};
    return __builtin_bit_cast(u32, v);
}
static __device__ __forceinline__ u32 lds_add(LAS u32* p, u32 v) { return __hip_atomic_fetch_add(p, v, __ATOMIC_RELAXED, __HIP_MEMORY_SCOPE_WORKGROUP); }
static __device__ __forceinline__ f32x4 mfma444(u32x2 a, u32x2 b, f32x4 c) {
    return __builtin_amdgcn_mfma_f32_4x4x4bf16_1k(__builtin_bit_cast(s16x4, a), __builtin_bit_cast(s16x4, b), c, 0, 0, 0);
}
template <int CTRL> static __device__ __forceinline__ float dppf(float v) {
    return __builtin_bit_cast(float, __builtin_amdgcn_update_dpp(0u, __builtin_bit_cast(u32, v), CTRL, 0xf, 0xf, true));
}
template <int CTRL> static __device__ __forceinline__ u32 dppu(u32 v) {
    return __builtin_amdgcn_update_dpp(0u, v, CTRL, 0xf, 0xf, true);
}
static __device__ __forceinline__ float diag4(f32x4 d) {
    float t = d[0] + dppf<0x55>(d[1]);
    t += dppf<0xAA>(d[2]);
    t += dppf<0xFF>(d[3]);
    return t;
}
struct Batch { u32x4 x0, x1, x2, x3, q; u32 node; };

static __device__ __forceinline__ void compute(const Batch& b, u32 m0, u32 m1, f32x4& acc0, f32x4& acc1) {
    const f32x4 z = {0.f, 0.f, 0.f, 0.f};
    const u32x2 ql = b.q.xy, qh = b.q.zw;
    f32x4 d0 = mfma444(b.x0.xy, ql, z), d1 = mfma444(b.x1.xy, ql, z), d2 = mfma444(b.x2.xy, ql, z), d3 = mfma444(b.x3.xy, ql, z);
    d0 = mfma444(b.x0.zw, qh, d0); d1 = mfma444(b.x1.zw, qh, d1); d2 = mfma444(b.x2.zw, qh, d2); d3 = mfma444(b.x3.zw, qh, d3);
    const float a = diag4(d0), bb = diag4(d1), c = diag4(d2), d = diag4(d3);
    float t0, t1, u;
    asm("s_nop 1\n\t"
        "v_add_f32_dpp %0, %3, %3 row_ror:8 row_mask:0xf bank_mask:0x3\n\t"
        "v_add_f32_dpp %1, %5, %5 row_ror:8 row_mask:0xf bank_mask:0x3\n\t"
        "v_add_f32_dpp %0, %4, %4 row_ror:8 row_mask:0xf bank_mask:0xc\n\t"
        "v_add_f32_dpp %1, %6, %6 row_ror:8 row_mask:0xf bank_mask:0xc\n\t"
        "s_nop 0\n\t"
        "v_add_f32_dpp %2, %0, %0 row_ror:12 row_mask:0xf bank_mask:0x5\n\t"
        "v_add_f32_dpp %2, %1, %1 row_ror:4 row_mask:0xf bank_mask:0xa"
        : "=&v"(t0), "=&v"(t1), "=&v"(u) : "v"(a), "v"(bb), "v"(c), "v"(d));
    const float al = __builtin_amdgcn_rcpf(1.0f + __builtin_amdgcn_exp2f(u));
    const u32 a2 = pk_bf16(al, al);
    { const u32x2 A = {dppu<0x150>(a2) & m0, dppu<0x150>(a2) & m1}; acc0 = mfma444(A, b.x0.xy, acc0); acc1 = mfma444(A, b.x0.zw, acc1); }
    { const u32x2 A = {dppu<0x158>(a2) & m0, dppu<0x158>(a2) & m1}; acc0 = mfma444(A, b.x1.xy, acc0); acc1 = mfma444(A, b.x1.zw, acc1); }
    { const u32x2 A = {dppu<0x154>(a2) & m0, dppu<0x154>(a2) & m1}; acc0 = mfma444(A, b.x2.xy, acc0); acc1 = mfma444(A, b.x2.zw, acc1); }
    { const u32x2 A = {dppu<0x15C>(a2) & m0, dppu<0x15C>(a2) & m1}; acc0 = mfma444(A, b.x3.xy, acc0); acc1 = mfma444(A, b.x3.zw, acc1); }
}

static __device__ __forceinline__ void wgemm(const LAS char* wimg, const LAS char* rows, int nt, int cg, int lane, f32x4& o0, f32x4& o1) {
    const int kg = lane >> 4, q = (lane >> 2) & 3, p = lane & 3, i16 = lane & 15;
    const int swz = ((q >> 1) << 2) | ((kg & 1) << 3);
    const int base = (8 * kg + q) * 256 + (((4 * cg + p) ^ swz) << 4);
    const int h0 = (q & 1) << 3, h1 = h0 ^ 8;
    o0 = (f32x4){0.f, 0.f, 0.f, 0.f}; o1 = o0;
#pragma unroll
    for (int s = 0; s < 4; ++s) {
        const bf16x8 bf = *(const LAS bf16x8*)(rows + (nt * 16 + i16) * 256 + (((4 * s + kg) ^ i16) << 4));
        const s16x4 a00 = __builtin_amdgcn_ds_read_tr16_b64_v4i16((LAS s16x4*)(wimg + base + h0 + s * 8192));
        const s16x4 a01 = __builtin_amdgcn_ds_read_tr16_b64_v4i16((LAS s16x4*)(wimg + base + h0 + s * 8192 + 1024));
        const s16x4 a10 = __builtin_amdgcn_ds_read_tr16_b64_v4i16((LAS s16x4*)(wimg + base + h1 + s * 8192));
        const s16x4 a11 = __builtin_amdgcn_ds_read_tr16_b64_v4i16((LAS s16x4*)(wimg + base + h1 + s * 8192 + 1024));
        const bf16x8 A0 = __builtin_bit_cast(bf16x8, __builtin_shufflevector(a00, a01, 0, 1, 2, 3, 4, 5, 6, 7));
        const bf16x8 A1 = __builtin_bit_cast(bf16x8, __builtin_shufflevector(a10, a11, 0, 1, 2, 3, 4, 5, 6, 7));
        o0 = __builtin_amdgcn_mfma_f32_16x16x32_bf16(A0, bf, o0, 0, 0, 0);
        o1 = __builtin_amdgcn_mfma_f32_16x16x32_bf16(A1, bf, o1, 0, 0, 0);
    }
}

static __device__ __forceinline__ u32x4 poll_piece(__amdgpu_buffer_rsrc_t rw, int off, u32x4 tg) {
    u32x4 v;
    for (int spin = 0; spin < (1 << 20); ++spin) {
        asm volatile("" ::: "memory");
        v = __builtin_amdgcn_raw_buffer_load_b128(rw, off, 0, 16);
        const u32 bad = ((v[0] ^ tg[0]) & TAG0) | (((v[1] ^ tg[1]) | (v[2] ^ tg[2]) | (v[3] ^ tg[3])) & TAG);
        if (bad == 0) break;
    }
    return v;
}

__global__ __launch_bounds__(1024) void k_graph(const char* __restrict__ x, const int* __restrict__ src, const int* __restrict__ dst,
                                                const char* __restrict__ wq, const char* __restrict__ wv,
                                                char* __restrict__ out, char* ws, int N, int E
) {
    __shared__ __attribute__((aligned(16))) char smem_[L_END];
    LAS char* smem = (LAS char*)smem_;
    const int tid = threadIdx.x, lane = tid & 63;
    const int w = __builtin_amdgcn_readfirstlane(tid >> 6);
    const int b = blockIdx.x;
    ST_DECL;
    ST(0);
    u32 pf = 0;
    {
        uint64_t pc; asm volatile("s_getpc_b64 %0" : "=s"(pc));
        const u32 off = (w * CODE_LPW + min(lane, CODE_LPW - 1)) * 128;
        asm volatile("global_load_dword %0, %1, %2" : "=v"(pf) : "v"(off), "s"(pc));
    }
    asm volatile("" :: "s"(x), "s"(src), "s"(dst), "s"(wq), "s"(wv), "s"(out), "s"(ws), "s"(N), "s"(E));
    const int CH = ((E + NB * 4 - 1) / (NB * 4)) * 4;
    const auto rx = __builtin_amdgcn_make_buffer_rsrc((void*)x, 0, N * 256, 0x00020000);
    const auto rd = __builtin_amdgcn_make_buffer_rsrc((void*)dst, 0, E * 4, 0x00020000);
    const auto rs = __builtin_amdgcn_make_buffer_rsrc((void*)src, 0, E * 4, 0x00020000);
    const auto rw = __builtin_amdgcn_make_buffer_rsrc((void*)ws, 0, WS_END, 0x00020000);
    const int g = lane >> 4, i16 = lane & 15;
    LAS u32* hist = (LAS u32*)(smem + L_HIST);
    LAS u32* cnts = (LAS u32*)(smem + L_CNT);
    LAS u32* ovf = (LAS u32*)(smem + L_OVF);
    LAS u32* list = (LAS u32*)(smem + L_LIST);

    u32x4 tg;
    {
        const u32x4 o = *(const __attribute__((address_space(4))) u32x4*)ws;
        const u32 c0 = (((o[0] >> 22) << 12) | ((o[1] >> 22) << 2) | ((o[1] >> 6) & 3)) + 1;
        const u32 c1 = (((o[2] >> 22) << 14) | (((o[2] >> 6) & 3) << 12) | ((o[3] >> 22) << 2) | ((o[3] >> 6) & 3)) + 1;
        tg[0] = (c0 >> 12) << 22;
        tg[1] = ((c0 >> 2) << 22) | ((c0 & 3) << 6);
        tg[2] = ((c1 >> 14) << 22) | (((c1 >> 12) & 3) << 6);
        tg[3] = ((c1 >> 2) << 22) | ((c1 & 3) << 6);
    }
    {
        const u32x4 none = {NONE, NONE, NONE, NONE};
        auto init = [&]() {
            LAS char* r = smem + L_STG + (tid >> 2) * STG_S + (tid & 3) * 16;
            *(LAS u32x4*)r = none; *(LAS u32x4*)(r + 64) = none;
            if (tid < 256 + 64 + 8) hist[tid] = 0;
        };
        if (w < 8) {
            const int e0 = b * CH + tid * 4;
            const bool have = tid * 4 < CH && e0 < E;
            const u32x4 d4 = __builtin_amdgcn_raw_buffer_load_b128(rd, have ? e0 * 4 : -16, 0, 0);
            const u32x4 s4 = __builtin_amdgcn_raw_buffer_load_b128(rs, have ? e0 * 4 : -16, 0, 0);
            init();
            __syncthreads();
            if (have) {
                u32 rank[4];
#pragma unroll
                for (int j = 0; j < 4; ++j) rank[j] = lds_add(&hist[d4[j] & 255], 1u);
#pragma unroll
                for (int j = 0; j < 4; ++j)
                    *(LAS u32*)(smem + L_STG + (d4[j] & 255) * STG_S + min(rank[j], 32u) * 4) = (s4[j] << 8) | (d4[j] >> 8);
            }
        } else {
            const int v = w - 8;
            u32x4 xr[2], wqv[4], wvv[4];
#pragma unroll
            for (int j = 0; j < 4; ++j) wqv[j] = *(const u32x4*)(wq + (16 * v + 4 * g + j) * 256 + i16 * 16);
#pragma unroll
            for (int j = 0; j < 2; ++j) xr[j] = __builtin_amdgcn_raw_buffer_load_b128(rx, ((8 * v + 2 * g + j) * 256 + b) * 256 + i16 * 16, 0, 0);
#pragma unroll
            for (int j = 0; j < 4; ++j) wvv[j] = *(const u32x4*)(wv + (16 * v + 4 * g + j) * 256 + i16 * 16);
            init();
            __syncthreads();
#pragma unroll
            for (int j = 0; j < 4; ++j) {
                const int o = (16 * v + 4 * g + j) * 256 + ((i16 ^ (((j >> 1) << 2) | ((g >> 1) << 3))) << 4);
                *(LAS u32x2*)(smem + L_WQ + o + (j & 1) * 8) = wqv[j].xy; *(LAS u32x2*)(smem + L_WQ + o + 8 - (j & 1) * 8) = wqv[j].zw;
                *(LAS u32x2*)(smem + L_WV + o + (j & 1) * 8) = wvv[j].xy; *(LAS u32x2*)(smem + L_WV + o + 8 - (j & 1) * 8) = wvv[j].zw;
            }
#pragma unroll
            for (int j = 0; j < 2; ++j) {
                const int ln = 8 * v + 2 * g + j;
                *(LAS u32x4*)(smem + L_XS + ln * 256 + ((i16 ^ (ln & 15)) << 4)) = xr[j];
            }
        }
    }
    asm volatile("s_waitcnt vmcnt(0)" : "+v"(pf));
    ST(1);
    __syncthreads();
    ST(2);
    {
        const LAS char* r = smem + L_STG + (tid >> 2) * STG_S + (tid & 3) * 16;
        const u32 c = hist[tid >> 2];
        u32x4 v = *(const LAS u32x4*)r | tg;
        v[0] |= (c > 16 ? F_MORE : 0u) | (c > 32 ? F_OVER : 0u);
        const int off = ((tid >> 2) * NB + b) * 64 + (tid & 3) * 16;
        __builtin_amdgcn_raw_buffer_store_b128(v, rw, WS_A + off, 0, 16);
        if (c > 16) __builtin_amdgcn_raw_buffer_store_b128(*(const LAS u32x4*)(r + 64) | tg, rw, WS_B + off, 0, 16);
    }
    ST(3);
    const int nt = w & 3, cg = w >> 2;
    {
        f32x4 o0, o1;
        wgemm(smem + L_WQ, smem + L_XS, nt, cg, lane, o0, o1);
        const float sc = -0.08838834764831845f * 1.4426950408889634f;
        o0 *= sc; o1 *= sc;
        const u32x4 qv = {pk_bf16(o0[0], o0[1]), pk_bf16(o0[2], o0[3]), pk_bf16(o1[0], o1[1]), pk_bf16(o1[2], o1[3])};
        *(LAS u32x4*)(smem + L_Q + (nt * 16 + i16) * 256 + (4 * cg + g) * 16) = qv;
    }
    ST(4);
    u32x4 ea = poll_piece(rw, WS_A + b * (NB * 64) + tid * 16, tg), eb = {NONE, NONE, NONE, NONE};
    bool more = (ea[0] & (F_MORE | F_OVER)) == F_MORE;
    if (more) eb = poll_piece(rw, WS_B + b * (NB * 64) + tid * 16, tg);
    if (ea[0] & F_OVER) {
        __hip_atomic_fetch_or(&ovf[tid >> 7], 1u << ((tid >> 2) & 31), __ATOMIC_RELAXED, __HIP_MEMORY_SCOPE_WORKGROUP);
        ea = eb;
    }
    ST(5);
    const u32 m0 = (lane & 3) == 0 ? 0x0000ffffu : (lane & 3) == 1 ? 0xffff0000u : 0u;
    const u32 m1 = (lane & 3) == 2 ? 0x0000ffffu : (lane & 3) == 3 ? 0xffff0000u : 0u;
    f32x4 tot0 = {0.f, 0.f, 0.f, 0.f}, tot1 = tot0;
    for (int round = 0;; ++round) {
        u32 ra[4], rb[4];
#pragma unroll
        for (int j = 0; j < 4; ++j) ra[j] = ((ea[j] & 63) != 63) ? lds_add(&cnts[ea[j] & 63], 1u) : 0u;
        if (more) {
#pragma unroll
            for (int j = 0; j < 4; ++j) rb[j] = ((eb[j] & 63) != 63) ? lds_add(&cnts[eb[j] & 63], 1u) : 0u;
        }
        __syncthreads();
        ST(7);
        if (round == 0 && b == 0 && tid == 0) __builtin_amdgcn_raw_buffer_store_b128(tg, rw, 0, 0, 16);
        const u32 c = cnts[lane];
        const u32 ov = lane < 8 ? ovf[lane] : 0u;
        const u32 nq = (c + 3) >> 2;
        const u32 inc = dpp_scan_incl(nq);
        const u32 s0 = (inc - nq) * 4;
        const int Tq = __builtin_amdgcn_readlane(inc, 63);
        u32 sj[4];
#pragma unroll
        for (int j = 0; j < 4; ++j) sj[j] = __builtin_amdgcn_ds_bpermute((ea[j] & 63) << 2, s0);
#pragma unroll
        for (int j = 0; j < 4; ++j) if ((ea[j] & 63) != 63) list[sj[j] + ra[j]] = ea[j];
        if (__any(more)) {
#pragma unroll
            for (int j = 0; j < 4; ++j) sj[j] = __builtin_amdgcn_ds_bpermute((eb[j] & 63) << 2, s0);
#pragma unroll
            for (int j = 0; j < 4; ++j) if ((eb[j] & 63) != 63) list[sj[j] + rb[j]] = eb[j];
        }
        const int n = (Tq + 63) >> 6;
        if (w == 0) {
#pragma unroll
            for (int j = 0; j < 3; ++j) if (c + j < nq * 4) list[s0 + c + j] = NONE;
        }
        if (w == 1) { for (int o = lane; o < 16 * n; o += 64) list[Tq * 4 + o] = NONE; }
        const unsigned long long ovb = __ballot(ov != 0);
        const u32 mg = n > 0 ? ((1u << 20) + n - 1) / n : 0u;
        const u32 myinc = __builtin_amdgcn_ds_bpermute((4 * w + g) << 2, inc), mynq = __builtin_amdgcn_ds_bpermute((4 * w + g) << 2, nq);
        __syncthreads();
        ST(8);
        if (4 * w * n < Tq) {
            const int gi = 4 * w + g;
            const LAS u32x4* lq = (const LAS u32x4*)list + gi * n;
            f32x4 acc0 = {0.f, 0.f, 0.f, 0.f}, acc1 = acc0;
            const u32 loff = i16 * 16;
            auto fetch = [&](Batch& B, const u32x4 e) {
                B.x0 = __builtin_amdgcn_raw_buffer_load_b128(rx, (e[0] & EMASK) | loff, 0, 0);
                B.x1 = __builtin_amdgcn_raw_buffer_load_b128(rx, (e[1] & EMASK) | loff, 0, 0);
                B.x2 = __builtin_amdgcn_raw_buffer_load_b128(rx, (e[2] & EMASK) | loff, 0, 0);
                B.x3 = __builtin_amdgcn_raw_buffer_load_b128(rx, (e[3] & EMASK) | loff, 0, 0);
                B.node = e[0] & 63;
                B.q = *(const LAS u32x4*)(smem + L_Q + B.node * 256 + loff);
            };
            u32 cur;
            LAS char* sl = smem + L_SLOT + gi * 512 + loff;
            auto flush = [&]() {
                *(LAS f32x4*)(sl + cur * 512) = acc0;
                *(LAS f32x4*)(sl + cur * 512 + 256) = acc1;
            };
            auto step = [&](const Batch& B) {
                if (B.node != cur) { flush(); acc0 = (f32x4){0.f, 0.f, 0.f, 0.f}; acc1 = acc0; cur = B.node; }
                compute(B, m0, m1, acc0, acc1);
            };
            Batch A, B;
            fetch(A, lq[0]);
            cur = A.node;
            u32x4 en = lq[1];
            int t = 1;
            for (; t + 1 < n; t += 2) {
                fetch(B, en); en = lq[t + 1]; step(A);
                fetch(A, en); en = lq[t + 2]; step(B);
            }
            if (t < n) { fetch(B, en); step(A); step(B); } else step(A);
            flush();
        }
        ST(9);
        __syncthreads();
        ST(10);
        if (mynq > 0) {
            const u32 gf = ((myinc - mynq) * mg) >> 20, gl = ((myinc - 1) * mg) >> 20;
            const LAS char* r = smem + L_SLOT + (4 * w + g) * 512 + i16 * 16;
            for (u32 gg = gf; gg <= gl; ++gg) {
                tot0 += *(const LAS f32x4*)(r + gg * 512);
                tot1 += *(const LAS f32x4*)(r + gg * 512 + 256);
            }
        }
        if (ovb == 0) break;
        const int wi = __builtin_ctzll(ovb);
        const u32 word = __builtin_amdgcn_readlane(ov, wi);
        const int bi = __builtin_ctz(word);
        const int e0 = (wi * 32 + bi) * CH + tid * 4;
        const bool have = tid * 4 < CH && e0 < E;
        const u32x4 d4 = __builtin_amdgcn_raw_buffer_load_b128(rd, e0 * 4, 0, 0), s4 = __builtin_amdgcn_raw_buffer_load_b128(rs, e0 * 4, 0, 0);
#pragma unroll
        for (int j = 0; j < 4; ++j) ea[j] = (have && (d4[j] & 255) == (u32)b) ? ((s4[j] << 8) | (d4[j] >> 8)) : NONE;
        more = false;
        eb = (u32x4){NONE, NONE, NONE, NONE};
        if (tid < 64) cnts[tid] = 0;
        if (tid == 0) ovf[wi] = word & (word - 1);
        __syncthreads();
    }
    int lane2 = lane;
    asm volatile("" : "+v"(lane2));
    {
        const int g = lane2 >> 4, i16 = lane2 & 15;
        const int ln = 4 * w + g;
        const u32x4 v = {pk_bf16(tot0[0], tot0[1]), pk_bf16(tot0[2], tot0[3]), pk_bf16(tot1[0], tot1[1]), pk_bf16(tot1[2], tot1[3])};
        *(LAS u32x4*)(smem + L_Q + ln * 256 + ((i16 ^ (ln & 15)) << 4)) = v;
    }
    __syncthreads();
    {
        f32x4 o0, o1;
        const int g = lane2 >> 4, i16 = lane2 & 15;
        wgemm(smem + L_WV, smem + L_Q, nt, cg, lane2, o0, o1);
        const u32x4 ov = {pk_bf16(o0[0], o0[1]), pk_bf16(o0[2], o0[3]), pk_bf16(o1[0], o1[1]), pk_bf16(o1[2], o1[3])};
        const int node = (nt * 16 + i16) * 256 + b;
        const auto ro = __builtin_amdgcn_make_buffer_rsrc((void*)out, 0, N * 256, 0x00020000);
        __builtin_amdgcn_raw_buffer_store_b128(ov, ro, node * 256 + (4 * cg + g) * 16, 0, 16);
    }
    ST(11);
    ST_END(0, 11);
}

extern "C" void kernel_launch(void* const* d_in, const int* in_sizes, int n_in,
                              void* d_out, int out_size, void* d_ws, size_t ws_size,
                              hipStream_t stream) {
    const int N = in_sizes[0] / 128, E = in_sizes[1];
    k_graph<<<NB, 1024, 0, stream>>>((const char*)d_in[0], (const int*)d_in[1], (const int*)d_in[2], (const char*)d_in[3], (const char*)d_in[4],
                                     (char*)d_out, (char*)d_ws, N, E
    );
}
```

```cpp
#include <hip/hip_runtime.h>
#include <stdint.h>

typedef uint32_t u32;
typedef __attribute__((ext_vector_type(4))) uint32_t u32x4;
typedef __attribute__((ext_vector_type(2))) uint32_t u32x2;
typedef __attribute__((ext_vector_type(4))) float f32x4;
typedef __attribute__((ext_vector_type(4))) short s16x4;
typedef __attribute__((ext_vector_type(8))) __bf16 bf16x8;
typedef __attribute__((ext_vector_type(2))) __bf16 bf16x2;
#define LAS __attribute__((address_space(3)))

#define ST_DECL
#define ST(i)
#define ST_END(lo, hi)
#define CODEPF 1
#define XPF 0

constexpr int CODE_LPW = 4;
constexpr int NB = 256;
constexpr u32 EMASK = 0x003FFF00u;
constexpr u32 NONE = EMASK | 63u;
constexpr u32 TAG0 = 0xFFC00000u, TAG = 0xFFC000C0u, F_MORE = 0x40u, F_OVER = 0x80u;
constexpr int WS_A = 4096, WS_B = WS_A + NB * NB * 64, WS_END = WS_B + NB * NB * 64;

constexpr int LIST_CAP = 8192 + 768;
constexpr int STG_S = 144;
constexpr int L_WV = 0, L_Q = 32768, L_SLOT = L_Q + 16384, L_STG = L_SLOT, L_XS = L_SLOT + 256 * STG_S, L_LIST = L_SLOT + 128 * 512, L_WQ = L_LIST;
constexpr int L_HIST = L_LIST + LIST_CAP * 4, L_CNT = L_HIST + 1024, L_OVF = L_CNT + 256, L_END = L_OVF + 32;

static __device__ __forceinline__ u32 dpp_scan_incl(u32 v) {
    v += __builtin_amdgcn_update_dpp(0u, v, 0x111, 0xf, 0xf, false);
    v += __builtin_amdgcn_update_dpp(0u, v, 0x112, 0xf, 0xf, false);
    v += __builtin_amdgcn_update_dpp(0u, v, 0x114, 0xf, 0xf, false);
    v += __builtin_amdgcn_update_dpp(0u, v, 0x118, 0xf, 0xf, false);
    v += __builtin_amdgcn_update_dpp(0u, v, 0x142, 0xa, 0xf, false);
    v += __builtin_amdgcn_update_dpp(0u, v, 0x143, 0xc, 0xf, false);
    return v;
}
static __device__ __forceinline__ u32 pk_bf16(float a, float b) {
    bf16x2 v = {(__bf16)a, (__bf16)b};
    return __builtin_bit_cast(u32, v);
}
static __device__ __forceinline__ u32 lds_add(LAS u32* p, u32 v) { return __hip_atomic_fetch_add(p, v, __ATOMIC_RELAXED, __HIP_MEMORY_SCOPE_WORKGROUP); }
static __device__ __forceinline__ f32x4 mfma444(u32x2 a, u32x2 b, f32x4 c) {
    return __builtin_amdgcn_mfma_f32_4x4x4bf16_1k(__builtin_bit_cast(s16x4, a), __builtin_bit_cast(s16x4, b), c, 0, 0, 0);
}
template <int CTRL> static __device__ __forceinline__ float dppf(float v) {
    return __builtin_bit_cast(float, __builtin_amdgcn_update_dpp(0u, __builtin_bit_cast(u32, v), CTRL, 0xf, 0xf, true));
}
template <int CTRL> static __device__ __forceinline__ u32 dppu(u32 v) {
    return __builtin_amdgcn_update_dpp(0u, v, CTRL, 0xf, 0xf, true);
}
static __device__ __forceinline__ float diag4(f32x4 d) {
    float t = d[0] + dppf<0x55>(d[1]);
    t += dppf<0xAA>(d[2]);
    t += dppf<0xFF>(d[3]);
    return t;
}
struct Batch { u32x4 x0, x1, x2, x3, q; u32 node; };

static __device__ __forceinline__ void compute(const Batch& b, u32 m0, u32 m1, f32x4& acc0, f32x4& acc1) {
    const f32x4 z = {0.f, 0.f, 0.f, 0.f};
    const u32x2 ql = b.q.xy, qh = b.q.zw;
    f32x4 d0 = mfma444(b.x0.xy, ql, z), d1 = mfma444(b.x1.xy, ql, z), d2 = mfma444(b.x2.xy, ql, z), d3 = mfma444(b.x3.xy, ql, z);
    d0 = mfma444(b.x0.zw, qh, d0); d1 = mfma444(b.x1.zw, qh, d1); d2 = mfma444(b.x2.zw, qh, d2); d3 = mfma444(b.x3.zw, qh, d3);
    const float a = diag4(d0), bb = diag4(d1), c = diag4(d2), d = diag4(d3);
    float t0, t1, u;
    asm("s_nop 1\n\t"
        "v_add_f32_dpp %0, %3, %3 row_ror:8 row_mask:0xf bank_mask:0x3\n\t"
        "v_add_f32_dpp %1, %5, %5 row_ror:8 row_mask:0xf bank_mask:0x3\n\t"
        "v_add_f32_dpp %0, %4, %4 row_ror:8 row_mask:0xf bank_mask:0xc\n\t"
        "v_add_f32_dpp %1, %6, %6 row_ror:8 row_mask:0xf bank_mask:0xc\n\t"
        "s_nop 0\n\t"
        "v_add_f32_dpp %2, %0, %0 row_ror:12 row_mask:0xf bank_mask:0x5\n\t"
        "v_add_f32_dpp %2, %1, %1 row_ror:4 row_mask:0xf bank_mask:0xa"
        : "=&v"(t0), "=&v"(t1), "=&v"(u) : "v"(a), "v"(bb), "v"(c), "v"(d));
    const float al = __builtin_amdgcn_rcpf(1.0f + __builtin_amdgcn_exp2f(u));
    const u32 a2 = pk_bf16(al, al);
    { const u32x2 A = {dppu<0x150>(a2) & m0, dppu<0x150>(a2) & m1}; acc0 = mfma444(A, b.x0.xy, acc0); acc1 = mfma444(A, b.x0.zw, acc1); }
    { const u32x2 A = {dppu<0x158>(a2) & m0, dppu<0x158>(a2) & m1}; acc0 = mfma444(A, b.x1.xy, acc0); acc1 = mfma444(A, b.x1.zw, acc1); }
    { const u32x2 A = {dppu<0x154>(a2) & m0, dppu<0x154>(a2) & m1}; acc0 = mfma444(A, b.x2.xy, acc0); acc1 = mfma444(A, b.x2.zw, acc1); }
    { const u32x2 A = {dppu<0x15C>(a2) & m0, dppu<0x15C>(a2) & m1}; acc0 = mfma444(A, b.x3.xy, acc0); acc1 = mfma444(A, b.x3.zw, acc1); }
}

static __device__ __forceinline__ void wgemm(const LAS char* wimg, const LAS char* rows, int nt, int cg, int lane, f32x4& o0, f32x4& o1) {
    const int kg = lane >> 4, q = (lane >> 2) & 3, p = lane & 3, i16 = lane & 15;
    const int swz = ((q >> 1) << 2) | ((kg & 1) << 3);
    const int base = (8 * kg + q) * 256 + (((4 * cg + p) ^ swz) << 4);
    const int h0 = (q & 1) << 3, h1 = h0 ^ 8;
    o0 = (f32x4){0.f, 0.f, 0.f, 0.f}; o1 = o0;
#pragma unroll
    for (int s = 0; s < 4; ++s) {
        const bf16x8 bf = *(const LAS bf16x8*)(rows + (nt * 16 + i16) * 256 + (((4 * s + kg) ^ i16) << 4));
        const s16x4 a00 = __builtin_amdgcn_ds_read_tr16_b64_v4i16((LAS s16x4*)(wimg + base + h0 + s * 8192));
        const s16x4 a01 = __builtin_amdgcn_ds_read_tr16_b64_v4i16((LAS s16x4*)(wimg + base + h0 + s * 8192 + 1024));
        const s16x4 a10 = __builtin_amdgcn_ds_read_tr16_b64_v4i16((LAS s16x4*)(wimg + base + h1 + s * 8192));
        const s16x4 a11 = __builtin_amdgcn_ds_read_tr16_b64_v4i16((LAS s16x4*)(wimg + base + h1 + s * 8192 + 1024));
        const bf16x8 A0 = __builtin_bit_cast(bf16x8, __builtin_shufflevector(a00, a01, 0, 1, 2, 3, 4, 5, 6, 7));
        const bf16x8 A1 = __builtin_bit_cast(bf16x8, __builtin_shufflevector(a10, a11, 0, 1, 2, 3, 4, 5, 6, 7));
        o0 = __builtin_amdgcn_mfma_f32_16x16x32_bf16(A0, bf, o0, 0, 0, 0);
        o1 = __builtin_amdgcn_mfma_f32_16x16x32_bf16(A1, bf, o1, 0, 0, 0);
    }
}

static __device__ __forceinline__ u32x4 poll_piece(__amdgpu_buffer_rsrc_t rw, int off, u32x4 tg) {
    u32x4 v;
    for (int spin = 0; spin < (1 << 20); ++spin) {
        asm volatile("" ::: "memory");
        v = __builtin_amdgcn_raw_buffer_load_b128(rw, off, 0, 16);
        const u32 bad = ((v[0] ^ tg[0]) & TAG0) | (((v[1] ^ tg[1]) | (v[2] ^ tg[2]) | (v[3] ^ tg[3])) & TAG);
        if (bad == 0) break;
    }
    return v;
}

typedef __attribute__((ext_vector_type(16))) uint32_t u32x16;
__global__ __launch_bounds__(1024) void k_graph(const char* __restrict__ x_, const int* __restrict__ src_, const int* __restrict__ dst_,
                                                const char* __restrict__ wq_, const char* __restrict__ wv_,
                                                char* __restrict__ out_, char* ws_, int N_, int E_) {
    unsigned long long tk0, tk1; u32x16 ka;
    asm volatile("s_memrealtime %0\n\ts_load_dwordx16 %2, %3, 0x0\n\ts_waitcnt lgkmcnt(0)\n\ts_memrealtime %1\n\ts_waitcnt lgkmcnt(0)" : "=&s"(tk0), "=&s"(tk1), "=&s"(ka) : "s"(__builtin_amdgcn_kernarg_segment_ptr()));
    #define GP(T, i) ((T)(__attribute__((address_space(1))) char*)(((uint64_t)ka[2 * (i) + 1] << 32) | ka[2 * (i)]))
    const char* __restrict__ x = GP(const char*, 0); const int* __restrict__ src = GP(const int*, 1); const int* __restrict__ dst = GP(const int*, 2);
    const char* __restrict__ wq = GP(const char*, 3); const char* __restrict__ wv = GP(const char*, 4); char* __restrict__ out = GP(char*, 5); char* ws = GP(char*, 6);
    const int N = ka[14], E = ka[15];
    __shared__ __attribute__((aligned(16))) char smem_[L_END];
    LAS char* smem = (LAS char*)smem_;
    const int tid = threadIdx.x, lane = tid & 63;
    const int w = __builtin_amdgcn_readfirstlane(tid >> 6);
    const int b = blockIdx.x;
    ST_DECL;
    ST(0);
    u32 pf = 0;
    {
        uint64_t pc; asm volatile("s_getpc_b64 %0" : "=s"(pc));
        const u32 off = (w * CODE_LPW + min(lane, CODE_LPW - 1)) * 128;
        asm volatile("global_load_dword %0, %1, %2" : "=v"(pf) : "v"(off), "s"(pc));
    }
    asm volatile("" :: "s"(x), "s"(src), "s"(dst), "s"(wq), "s"(wv), "s"(out), "s"(ws), "s"(N), "s"(E));
    const int CH = ((E + NB * 4 - 1) / (NB * 4)) * 4;
    const auto rx = __builtin_amdgcn_make_buffer_rsrc((void*)x, 0, N * 256, 0x00020000);
    const auto rd = __builtin_amdgcn_make_buffer_rsrc((void*)dst, 0, E * 4, 0x00020000);
    const auto rs = __builtin_amdgcn_make_buffer_rsrc((void*)src, 0, E * 4, 0x00020000);
    const auto rw = __builtin_amdgcn_make_buffer_rsrc((void*)ws, 0, WS_END, 0x00020000);
    const int g = lane >> 4, i16 = lane & 15;
    LAS u32* hist = (LAS u32*)(smem + L_HIST);
    LAS u32* cnts = (LAS u32*)(smem + L_CNT);
    LAS u32* ovf = (LAS u32*)(smem + L_OVF);
    LAS u32* list = (LAS u32*)(smem + L_LIST);

    u32x4 tg;
    {
        const u32x4 o = *(const __attribute__((address_space(4))) u32x4*)ws;
        const u32 c0 = (((o[0] >> 22) << 12) | ((o[1] >> 22) << 2) | ((o[1] >> 6) & 3)) + 1;
        const u32 c1 = (((o[2] >> 22) << 14) | (((o[2] >> 6) & 3) << 12) | ((o[3] >> 22) << 2) | ((o[3] >> 6) & 3)) + 1;
        tg[0] = (c0 >> 12) << 22;
        tg[1] = ((c0 >> 2) << 22) | ((c0 & 3) << 6);
        tg[2] = ((c1 >> 14) << 22) | (((c1 >> 12) & 3) << 6);
        tg[3] = ((c1 >> 2) << 22) | ((c1 & 3) << 6);
    }
    {
        const u32x4 none = {NONE, NONE, NONE, NONE};
        auto init = [&]() {
            LAS char* r = smem + L_STG + (tid >> 2) * STG_S + (tid & 3) * 16;
            *(LAS u32x4*)r = none; *(LAS u32x4*)(r + 64) = none;
            if (tid < 256 + 64 + 8) hist[tid] = 0;
        };
        if (w < 8) {
            const int e0 = b * CH + tid * 4;
            const bool have = tid * 4 < CH && e0 < E;
            const u32x4 d4 = __builtin_amdgcn_raw_buffer_load_b128(rd, have ? e0 * 4 : -16, 0, 0);
            const u32x4 s4 = __builtin_amdgcn_raw_buffer_load_b128(rs, have ? e0 * 4 : -16, 0, 0);
            init();
            __syncthreads();
            if (have) {
                u32 rank[4];
#pragma unroll
                for (int j = 0; j < 4; ++j) rank[j] = lds_add(&hist[d4[j] & 255], 1u);
#pragma unroll
                for (int j = 0; j < 4; ++j)
                    *(LAS u32*)(smem + L_STG + (d4[j] & 255) * STG_S + min(rank[j], 32u) * 4) = (s4[j] << 8) | (d4[j] >> 8);
            }
        } else {
            const int v = w - 8;
            u32x4 xr[2], wqv[4], wvv[4];
#pragma unroll
            for (int j = 0; j < 4; ++j) wqv[j] = *(const u32x4*)(wq + (16 * v + 4 * g + j) * 256 + i16 * 16);
#pragma unroll
            for (int j = 0; j < 2; ++j) xr[j] = __builtin_amdgcn_raw_buffer_load_b128(rx, ((8 * v + 2 * g + j) * 256 + b) * 256 + i16 * 16, 0, 0);
#pragma unroll
            for (int j = 0; j < 4; ++j) wvv[j] = *(const u32x4*)(wv + (16 * v + 4 * g + j) * 256 + i16 * 16);
            init();
            __syncthreads();
#pragma unroll
            for (int j = 0; j < 4; ++j) {
                const int o = (16 * v + 4 * g + j) * 256 + ((i16 ^ (((j >> 1) << 2) | ((g >> 1) << 3))) << 4);
                *(LAS u32x2*)(smem + L_WQ + o + (j & 1) * 8) = wqv[j].xy; *(LAS u32x2*)(smem + L_WQ + o + 8 - (j & 1) * 8) = wqv[j].zw;
                *(LAS u32x2*)(smem + L_WV + o + (j & 1) * 8) = wvv[j].xy; *(LAS u32x2*)(smem + L_WV + o + 8 - (j & 1) * 8) = wvv[j].zw;
            }
#pragma unroll
            for (int j = 0; j < 2; ++j) {
                const int ln = 8 * v + 2 * g + j;
                *(LAS u32x4*)(smem + L_XS + ln * 256 + ((i16 ^ (ln & 15)) << 4)) = xr[j];
            }
        }
    }
    asm volatile("s_waitcnt vmcnt(0)" : "+v"(pf));
    ST(1);
    __syncthreads();
    ST(2);
    {
        const LAS char* r = smem + L_STG + (tid >> 2) * STG_S + (tid & 3) * 16;
        const u32 c = hist[tid >> 2];
        u32x4 v = *(const LAS u32x4*)r | tg;
        v[0] |= (c > 16 ? F_MORE : 0u) | (c > 32 ? F_OVER : 0u);
        const int off = ((tid >> 2) * NB + b) * 64 + (tid & 3) * 16;
        __builtin_amdgcn_raw_buffer_store_b128(v, rw, WS_A + off, 0, 16);
        if (c > 16) __builtin_amdgcn_raw_buffer_store_b128(*(const LAS u32x4*)(r + 64) | tg, rw, WS_B + off, 0, 16);
    }
    ST(3);
    const int nt = w & 3, cg = w >> 2;
    {
        f32x4 o0, o1;
        wgemm(smem + L_WQ, smem + L_XS, nt, cg, lane, o0, o1);
        const float sc = -0.08838834764831845f * 1.4426950408889634f;
        o0 *= sc; o1 *= sc;
        const u32x4 qv = {pk_bf16(o0[0], o0[1]), pk_bf16(o0[2], o0[3]), pk_bf16(o1[0], o1[1]), pk_bf16(o1[2], o1[3])};
        *(LAS u32x4*)(smem + L_Q + (nt * 16 + i16) * 256 + (4 * cg + g) * 16) = qv;
    }
    ST(4);
    u32x4 ea = poll_piece(rw, WS_A + b * (NB * 64) + tid * 16, tg), eb = {NONE, NONE, NONE, NONE};
    bool more = (ea[0] & (F_MORE | F_OVER)) == F_MORE;
    if (more) eb = poll_piece(rw, WS_B + b * (NB * 64) + tid * 16, tg);
    if (ea[0] & F_OVER) {
        __hip_atomic_fetch_or(&ovf[tid >> 7], 1u << ((tid >> 2) & 31), __ATOMIC_RELAXED, __HIP_MEMORY_SCOPE_WORKGROUP);
        ea = eb;
    }
    ST(5);
    const u32 m0 = (lane & 3) == 0 ? 0x0000ffffu : (lane & 3) == 1 ? 0xffff0000u : 0u;
    const u32 m1 = (lane & 3) == 2 ? 0x0000ffffu : (lane & 3) == 3 ? 0xffff0000u : 0u;
    f32x4 tot0 = {0.f, 0.f, 0.f, 0.f}, tot1 = tot0;
    for (int round = 0;; ++round) {
        u32 ra[4], rb[4];
#pragma unroll
        for (int j = 0; j < 4; ++j) ra[j] = ((ea[j] & 63) != 63) ? lds_add(&cnts[ea[j] & 63], 1u) : 0u;
        if (more) {
#pragma unroll
            for (int j = 0; j < 4; ++j) rb[j] = ((eb[j] & 63) != 63) ? lds_add(&cnts[eb[j] & 63], 1u) : 0u;
        }
        __syncthreads();
        ST(7);
        if (round == 0 && b == 0 && tid == 0) __builtin_amdgcn_raw_buffer_store_b128(tg, rw, 0, 0, 16);
        const u32 c = cnts[lane];
        const u32 ov = lane < 8 ? ovf[lane] : 0u;
        const u32 nq = (c + 3) >> 2;
        const u32 inc = dpp_scan_incl(nq);
        const u32 s0 = (inc - nq) * 4;
        const int Tq = __builtin_amdgcn_readlane(inc, 63);
        u32 sj[4];
#pragma unroll
        for (int j = 0; j < 4; ++j) sj[j] = __builtin_amdgcn_ds_bpermute((ea[j] & 63) << 2, s0);
#pragma unroll
        for (int j = 0; j < 4; ++j) if ((ea[j] & 63) != 63) list[sj[j] + ra[j]] = ea[j];
        if (__any(more)) {
#pragma unroll
            for (int j = 0; j < 4; ++j) sj[j] = __builtin_amdgcn_ds_bpermute((eb[j] & 63) << 2, s0);
#pragma unroll
            for (int j = 0; j < 4; ++j) if ((eb[j] & 63) != 63) list[sj[j] + rb[j]] = eb[j];
        }
        const int n = (Tq + 63) >> 6;
        if (w == 0) {
#pragma unroll
            for (int j = 0; j < 3; ++j) if (c + j < nq * 4) list[s0 + c + j] = NONE;
        }
        if (w == 1) { for (int o = lane; o < 16 * n; o += 64) list[Tq * 4 + o] = NONE; }
        const unsigned long long ovb = __ballot(ov != 0);
        const u32 mg = n > 0 ? ((1u << 20) + n - 1) / n : 0u;
        const u32 myinc = __builtin_amdgcn_ds_bpermute((4 * w + g) << 2, inc), mynq = __builtin_amdgcn_ds_bpermute((4 * w + g) << 2, nq);
        __syncthreads();
        ST(8);
        if (4 * w * n < Tq) {
            const int gi = 4 * w + g;
            const LAS u32x4* lq = (const LAS u32x4*)list + gi * n;
            f32x4 acc0 = {0.f, 0.f, 0.f, 0.f}, acc1 = acc0;
            const u32 loff = i16 * 16;
            auto fetch = [&](Batch& B, const u32x4 e) {
                B.x0 = __builtin_amdgcn_raw_buffer_load_b128(rx, (e[0] & EMASK) | loff, 0, 0);
                B.x1 = __builtin_amdgcn_raw_buffer_load_b128(rx, (e[1] & EMASK) | loff, 0, 0);
                B.x2 = __builtin_amdgcn_raw_buffer_load_b128(rx, (e[2] & EMASK) | loff, 0, 0);
                B.x3 = __builtin_amdgcn_raw_buffer_load_b128(rx, (e[3] & EMASK) | loff, 0, 0);
                B.node = e[0] & 63;
                B.q = *(const LAS u32x4*)(smem + L_Q + B.node * 256 + loff);
            };
            u32 cur;
            LAS char* sl = smem + L_SLOT + gi * 512 + loff;
            auto flush = [&]() {
                *(LAS f32x4*)(sl + cur * 512) = acc0;
                *(LAS f32x4*)(sl + cur * 512 + 256) = acc1;
            };
            auto step = [&](const Batch& B) {
                if (B.node != cur) { flush(); acc0 = (f32x4){0.f, 0.f, 0.f, 0.f}; acc1 = acc0; cur = B.node; }
                compute(B, m0, m1, acc0, acc1);
            };
            Batch A, B;
            fetch(A, lq[0]);
            cur = A.node;
            u32x4 en = lq[1];
            int t = 1;
            for (; t + 1 < n; t += 2) {
                fetch(B, en); en = lq[t + 1]; step(A);
                fetch(A, en); en = lq[t + 2]; step(B);
            }
            if (t < n) { fetch(B, en); step(A); step(B); } else step(A);
            flush();
        }
        ST(9);
        __syncthreads();
        ST(10);
        if (mynq > 0) {
            const u32 gf = ((myinc - mynq) * mg) >> 20, gl = ((myinc - 1) * mg) >> 20;
            const LAS char* r = smem + L_SLOT + (4 * w + g) * 512 + i16 * 16;
            for (u32 gg = gf; gg <= gl; ++gg) {
                tot0 += *(const LAS f32x4*)(r + gg * 512);
                tot1 += *(const LAS f32x4*)(r + gg * 512 + 256);
            }
        }
        if (ovb == 0) break;
        const int wi = __builtin_ctzll(ovb);
        const u32 word = __builtin_amdgcn_readlane(ov, wi);
        const int bi = __builtin_ctz(word);
        const int e0 = (wi * 32 + bi) * CH + tid * 4;
        const bool have = tid * 4 < CH && e0 < E;
        const u32x4 d4 = __builtin_amdgcn_raw_buffer_load_b128(rd, e0 * 4, 0, 0), s4 = __builtin_amdgcn_raw_buffer_load_b128(rs, e0 * 4, 0, 0);
#pragma unroll
        for (int j = 0; j < 4; ++j) ea[j] = (have && (d4[j] & 255) == (u32)b) ? ((s4[j] << 8) | (d4[j] >> 8)) : NONE;
        more = false;
        eb = (u32x4){NONE, NONE, NONE, NONE};
        if (tid < 64) cnts[tid] = 0;
        if (tid == 0) ovf[wi] = word & (word - 1);
        __syncthreads();
    }
    int lane2 = lane;
    asm volatile("" : "+v"(lane2));
    {
        const int g = lane2 >> 4, i16 = lane2 & 15;
        const int ln = 4 * w + g;
        const u32x4 v = {pk_bf16(tot0[0], tot0[1]), pk_bf16(tot0[2], tot0[3]), pk_bf16(tot1[0], tot1[1]), pk_bf16(tot1[2], tot1[3])};
        *(LAS u32x4*)(smem + L_Q + ln * 256 + ((i16 ^ (ln & 15)) << 4)) = v;
    }
    __syncthreads();
    {
        f32x4 o0, o1;
        const int g = lane2 >> 4, i16 = lane2 & 15;
        wgemm(smem + L_WV, smem + L_Q, nt, cg, lane2, o0, o1);
        const u32x4 ov = {pk_bf16(o0[0], o0[1]), pk_bf16(o0[2], o0[3]), pk_bf16(o1[0], o1[1]), pk_bf16(o1[2], o1[3])};
        const int node = (nt * 16 + i16) * 256 + b;
        if (node < N) *(u32x4*)(out + (size_t)node * 256 + (4 * cg + g) * 16) = ov;
    }
    ST(11);
}

extern "C" void kernel_launch(void* const* d_in, const int* in_sizes, int n_in,
                              void* d_out, int out_size, void* d_ws, size_t ws_size,
                              hipStream_t stream) {
    const int N = in_sizes[0] / 128, E = in_sizes[1];
    k_graph<<<NB, 1024, 0, stream>>>((const char*)d_in[0], (const int*)d_in[1], (const int*)d_in[2], (const char*)d_in[3], (const char*)d_in[4],
                                     (char*)d_out, (char*)d_ws, N, E
    );
}
```

```cpp
#define TOUCHOFF ((lane & 3) << 20)
#include <hip/hip_runtime.h>
#include <stdint.h>

typedef uint32_t u32;
typedef __attribute__((ext_vector_type(4))) uint32_t u32x4;
typedef __attribute__((ext_vector_type(2))) uint32_t u32x2;
typedef __attribute__((ext_vector_type(4))) float f32x4;
typedef __attribute__((ext_vector_type(4))) short s16x4;
typedef __attribute__((ext_vector_type(8))) __bf16 bf16x8;
typedef __attribute__((ext_vector_type(2))) __bf16 bf16x2;
#define LAS __attribute__((address_space(3)))

#define ST_DECL
#define ST(i)
#define ST_END(lo, hi)
#define CODEPF 1
#define EDGE_MOD " nt"
#define OUT_AUX 16
#define USEPRIO 1
#define PRIO_A "2"
#define PRIO_B "4"
#define POLL_SLEEP 0
#define XPF 0
#define XPF_N 1

constexpr int CODE_LINES = 56;
constexpr int NB = 256;
constexpr u32 EMASK = 0x003FFF00u, NMASK = 0xFCu;
constexpr u32 NONE = EMASK | NMASK;
constexpr u32 TAG0 = 0xFFC00000u, TAG = 0xFFC00003u, F_MORE = 1u, F_OVER = 2u;
constexpr int WS_A = 4096, WS_B = WS_A + NB * NB * 64, WS_END = WS_B + NB * NB * 64;

constexpr int STG_S = 144;
constexpr int ROW_S = 272, SLOT_S = 2 * ROW_S;
constexpr int L_Q = 0, L_HIST = L_Q, L_AGG = L_Q, L_LIST = 64 * ROW_S, L_CNT = L_LIST + 16384, L_SEG = L_CNT + 256, L_ARR = L_SEG + 256, L_ZERO = L_ARR + 16, L_SLOT = L_ARR + 512;
constexpr int L_STG = L_SLOT, L_XS = L_SLOT + 256 * STG_S, L_WQ = L_XS + 16384, L_WV = L_WQ + 32768, L_END = L_WV + 32768;
constexpr int FIX0 = 95;
static_assert(L_XS % 256 == 0 && L_WQ % 256 == 0, "addresses are formed with xor");
static_assert(L_SLOT + (FIX0 + 32) * SLOT_S <= L_WV, "slots");

static __device__ __forceinline__ u32 dpp_scan_incl(u32 v) {
    v += __builtin_amdgcn_update_dpp(0u, v, 0x111, 0xf, 0xf, false);
    v += __builtin_amdgcn_update_dpp(0u, v, 0x112, 0xf, 0xf, false);
    v += __builtin_amdgcn_update_dpp(0u, v, 0x114, 0xf, 0xf, false);
    v += __builtin_amdgcn_update_dpp(0u, v, 0x118, 0xf, 0xf, false);
    v += __builtin_amdgcn_update_dpp(0u, v, 0x142, 0xa, 0xf, false);
    v += __builtin_amdgcn_update_dpp(0u, v, 0x143, 0xc, 0xf, false);
    return v;
}
static __device__ __forceinline__ u32 pk_bf16(float a, float b) {
    bf16x2 v = {(__bf16)a, (__bf16)b};
    return __builtin_bit_cast(u32, v);
}
static __device__ __forceinline__ u32 lds_add(LAS u32* p, u32 v) { return __hip_atomic_fetch_add(p, v, __ATOMIC_RELAXED, __HIP_MEMORY_SCOPE_WORKGROUP); }
static __device__ __forceinline__ f32x4 mfma444(u32x2 a, u32x2 b, f32x4 c) {
    return __builtin_amdgcn_mfma_f32_4x4x4bf16_1k(__builtin_bit_cast(s16x4, a), __builtin_bit_cast(s16x4, b), c, 0, 0, 0);
}
template <int CTRL> static __device__ __forceinline__ float dppf(float v) {
    return __builtin_bit_cast(float, __builtin_amdgcn_update_dpp(0u, __builtin_bit_cast(u32, v), CTRL, 0xf, 0xf, true));
}
template <int CTRL> static __device__ __forceinline__ u32 dppu(u32 v) {
    return __builtin_amdgcn_update_dpp(0u, v, CTRL, 0xf, 0xf, true);
}
static __device__ __forceinline__ float diag4(f32x4 d) {
    float t = d[0] + dppf<0x55>(d[1]);
    t += dppf<0xAA>(d[2]);
    t += dppf<0xFF>(d[3]);
    return t;
}
struct Quad { u32x4 x0, x1, q0, q1; u32 row; };
static __device__ __forceinline__ f32x4 mfma16(u32x4 a, u32x4 b, f32x4 c) {
    return __builtin_amdgcn_mfma_f32_16x16x32_bf16(__builtin_bit_cast(bf16x8, a), __builtin_bit_cast(bf16x8, b), c, 0, 0, 0);
}
static __device__ __forceinline__ u32 alphas(const Quad& a, const Quad& b) {
    const f32x4 z = {0.f, 0.f, 0.f, 0.f};
    f32x4 da = mfma444(a.x0.xy, a.q0.xy, z), db = mfma444(b.x0.xy, b.q0.xy, z);
    da = mfma444(a.x0.zw, a.q0.zw, da); db = mfma444(b.x0.zw, b.q0.zw, db);
    da = mfma444(a.x1.xy, a.q1.xy, da); db = mfma444(b.x1.xy, b.q1.xy, db);
    da = mfma444(a.x1.zw, a.q1.zw, da); db = mfma444(b.x1.zw, b.q1.zw, db);
    const float ta = diag4(da), tb = diag4(db);
    float u;
    asm("s_nop 1\n\t"
        "v_add_f32_dpp %0, %1, %1 row_shl:4 row_mask:0xf bank_mask:0x5\n\t"
        "v_add_f32_dpp %0, %2, %2 row_shr:4 row_mask:0xf bank_mask:0xa\n\t"
        "s_nop 1\n\t"
        "v_exp_f32_dpp %0, %0 quad_perm:[0,0,0,0] row_mask:0xf bank_mask:0xf\n\t"
        "s_nop 0"
        : "=&v"(u) : "v"(ta), "v"(tb));
    const float al = __builtin_amdgcn_rcpf(1.0f + u);
    return pk_bf16(al, al);
}

struct WFrag { bf16x8 a0[4], a1[4]; };
static __device__ __forceinline__ void wfrag(const LAS char* p0, const LAS char* p1, WFrag& f) {
#pragma unroll
    for (int s = 0; s < 4; ++s) {
        const s16x4 a00 = __builtin_amdgcn_ds_read_tr16_b64_v4i16((LAS s16x4*)(p0 + s * 8192));
        const s16x4 a01 = __builtin_amdgcn_ds_read_tr16_b64_v4i16((LAS s16x4*)(p0 + s * 8192 + 1024));
        const s16x4 a10 = __builtin_amdgcn_ds_read_tr16_b64_v4i16((LAS s16x4*)(p1 + s * 8192));
        const s16x4 a11 = __builtin_amdgcn_ds_read_tr16_b64_v4i16((LAS s16x4*)(p1 + s * 8192 + 1024));
        f.a0[s] = __builtin_bit_cast(bf16x8, __builtin_shufflevector(a00, a01, 0, 1, 2, 3, 4, 5, 6, 7));
        f.a1[s] = __builtin_bit_cast(bf16x8, __builtin_shufflevector(a10, a11, 0, 1, 2, 3, 4, 5, 6, 7));
    }
}
static __device__ __forceinline__ void wgemm(const WFrag& f, const bf16x8 (&bf)[4], f32x4& o0, f32x4& o1) {
    o0 = (f32x4){0.f, 0.f, 0.f, 0.f}; o1 = o0;
#pragma unroll
    for (int s = 0; s < 4; ++s) {
        o0 = __builtin_amdgcn_mfma_f32_16x16x32_bf16(f.a0[s], bf[s], o0, 0, 0, 0);
        o1 = __builtin_amdgcn_mfma_f32_16x16x32_bf16(f.a1[s], bf[s], o1, 0, 0, 0);
    }
}
static __device__ __forceinline__ u32x4 srd(const void* p, u32 bytes) {
    const uint64_t a = (uint64_t)p;
    return (u32x4){(u32)a, (u32)(a >> 32) & 0xffffu, bytes, 0x00020000u};
}
static __device__ __forceinline__ void load_w(u32x4 (&v)[4], u32x4 r, u32 va, u32 vb) {
    asm volatile("buffer_load_dwordx4 %0, %4, %6, 0 offen\n\tbuffer_load_dwordx4 %1, %4, %6, 0 offen offset:1024\n\t"
                 "buffer_load_dwordx4 %2, %5, %6, 0 offen offset:2048\n\tbuffer_load_dwordx4 %3, %5, %6, 0 offen offset:3072"
                 : "=&v"(v[0]), "=&v"(v[1]), "=&v"(v[2]), "=&v"(v[3]) : "v"(va), "v"(vb), "s"(r));
}

static __device__ __forceinline__ u32x4 load_piece(__amdgpu_buffer_rsrc_t rw, u32 voff, u32 soff) {
    asm volatile("" ::: "memory");
    return __builtin_amdgcn_raw_buffer_load_b128(rw, voff, soff, 16);
}
static __device__ __forceinline__ u32x4 poll_piece(u32x4 v, __amdgpu_buffer_rsrc_t rw, u32 voff, u32 soff, u32x4 tg) {
    for (int spin = 0; spin < (1 << 20); ++spin) {
        const u32 bad = ((v[0] ^ tg[0]) & TAG0) | (((v[1] ^ tg[1]) | (v[2] ^ tg[2]) | (v[3] ^ tg[3])) & TAG);
        if (bad == 0) break;
        if (POLL_SLEEP) __builtin_amdgcn_s_sleep(POLL_SLEEP);
        v = load_piece(rw, voff, soff);
    }
    return v;
}
static __device__ __forceinline__ u32 and_or(u32 a, u32 m, u32 c) { return __builtin_amdgcn_bitop3_b32(a, m, c, 0xEA); }
#define PIN(...) asm volatile("" : __VA_ARGS__)

__global__ __launch_bounds__(1024) void k_graph(const char* __restrict__ x, const int* __restrict__ src, const int* __restrict__ dst,
                                                const char* __restrict__ wq, const char* __restrict__ wv,
                                                char* __restrict__ out, char* ws, int N, int E
) {
    __shared__ __attribute__((aligned(16))) char smem_[L_END];
    LAS char* smem = (LAS char*)smem_;
    const int tid = threadIdx.x, lane = tid & 63;
    const int w = __builtin_amdgcn_readfirstlane(tid >> 6);
    const int b = blockIdx.x;
    u32 pf = 0;
    if (w == 7) {
        uint64_t pc; asm volatile("s_getpc_b64 %0" : "=s"(pc));
        asm volatile("global_load_dword %0, %1, %2" : "=v"(pf) : "v"(min(lane, CODE_LINES - 1) * 128), "s"(pc));
    }
    asm volatile("" :: "s"(x), "s"(src), "s"(dst), "s"(wq), "s"(wv), "s"(out), "s"(ws), "s"(N), "s"(E));
    ST_DECL;
    ST(0);
    const int CH = ((E + NB * 4 - 1) >> 10) * 4;
    const auto rw = __builtin_amdgcn_make_buffer_rsrc((void*)ws, 0, WS_END, 0x00020000);

    const u32x4 none = {NONE, NONE, NONE, NONE};
    const u32 t16 = tid * 16;
    const int ne = min(CH, E - b * CH);
    const bool binner = w * 256 < ne;
    u32x4 d4, s4;
    if (binner)
        asm volatile("buffer_load_dwordx4 %0, %2, %3, 0 offen" EDGE_MOD "\n\tbuffer_load_dwordx4 %1, %2, %4, 0 offen" EDGE_MOD
                     : "=&v"(d4), "=&v"(s4) : "v"(t16), "s"(srd(dst + b * CH, ne * 4)), "s"(srd(src + b * CH, ne * 4)));
    const u32x4 old = *(const __attribute__((address_space(4))) u32x4*)ws;
    const u32 t2 = tid >> 2, k16 = t16 & 48;
    u32 stg = t2 * STG_S + k16, poff = (t2 << 14) | k16, hoff = tid & 0x3fc, four = 4;
    *(LAS u32x4*)(smem + L_STG + stg) = none;
    if (w < 4) { *(LAS u32*)(smem + L_HIST + tid * 4) = 0; if (w == 0) { *(LAS u32*)(smem + L_CNT + tid * 4) = 0; *(LAS u32*)(smem + L_ARR + tid * 4) = 0; *(LAS u32*)(smem + L_ARR + 256 + (tid & 15) * 4) = 0; } }
    __syncthreads();
    const u32 c16 = t16 & 0xf0, g16 = tid & 48;
    u32x4 wr[4], xr0, xr1;
    if (w >= 8) {
        const int v = w - 8;
        const u32 wva = (c16 ^ (g16 << 2)) + (g16 << 4) + v * 4096;
        load_w(wr, srd(wq, 32768), wva, wva ^ 16);
        const u32 xa = ((c16 ^ g16) ^ ((v & 1) << 7)) + (g16 << 12) + (v << 19);
        if (v * 2048 + b < N)
            asm volatile("buffer_load_dwordx4 %0, %2, %4, 0 offen\n\tbuffer_load_dwordx4 %1, %3, %4, 0 offen"
                         : "=&v"(xr0), "=&v"(xr1) : "v"(xa), "v"((xa ^ 64) + (4 << 16)), "s"(srd(x + b * 256, (N - b) * 256)));
    }
    u32 xp0 = 0, xp1 = 0;
    auto xtouch = [&]() {
        const int v = w - 8, b2 = (b & ~7) | ((b + 1 + (v >> 1)) & 7);
        asm volatile("buffer_load_dword %0, %1, %2, 0 offen" : "=v"(xp0) : "v"(v < 2 * XPF_N ? lane << 16 : -4), "s"(srd(x + b2 * 256 + (v & 1) * 128, (N - b2) * 256 - 128)));
    };
    if (XPF == 1 && w >= 8) xtouch();
    u32 tch = 0;
    if (w == 7) asm volatile("buffer_load_dword %0, %1, %2, 0 offen" : "=v"(tch) : "v"(TOUCHOFF), "s"(srd(out + b * 256, (N - b) * 256)));
    u32x4 tg;
    {
        const uint64_t c0 = (((uint64_t)(old[0] | ~TAG0) << 32) | (old[1] | ~TAG)) + 1, c1 = (((uint64_t)(old[2] | ~TAG) << 32) | (old[3] | ~TAG)) + 1;
        tg[0] = (u32)(c0 >> 32) & TAG0; tg[1] = (u32)c0 & TAG; tg[2] = (u32)(c1 >> 32) & TAG; tg[3] = (u32)c1 & TAG;
    }
    const int nt = w & 3, cg = w >> 2;
    u32 wvp = ((c16 ^ (tid & 16) ^ (cg << 6)) | ((tid & 12) << 6) | (g16 << 7)) + L_WQ;
    u32 wq0 = wvp | ((tid & 1) << 3), wq1 = wq0 ^ 8;
    u32 xsp = ((c16 << 4) | (c16 ^ g16)) + (L_XS + nt * 4096);
    u32 agr = (lane & 15) * ROW_S + g16 + (L_Q + nt * 16 * ROW_S);
    u32 qw0 = agr + ((tid & 16) >> 1) + cg * 64, qw1 = qw0 ^ 8;
    u32 pso = WS_A + (((b & 7) << 5) | (b >> 3)) * 64;
    PIN("+s"(tg), "+s"(pso), "+v"(poff), "+v"(hoff), "+v"(stg), "+v"(four), "+v"(wvp), "+v"(wq0), "+v"(wq1), "+v"(xsp), "+v"(qw0), "+v"(qw1), "+v"(agr));
    if (binner) {
        asm volatile("s_waitcnt vmcnt(0)" : "+v"(d4), "+v"(s4));
        if (tid * 4 < ne) {
            u32 rank[4];
#pragma unroll
            for (int j = 0; j < 4; ++j) rank[j] = lds_add((LAS u32*)(smem + L_HIST + (d4[j] & 255) * 4), four);
#pragma unroll
            for (int j = 0; j < 4; ++j)
                *(LAS u32*)(smem + L_STG + (d4[j] & 255) * STG_S + min(rank[j], 128u)) = (s4[j] << 8) | ((d4[j] >> 6) & NMASK);
        }
    }
    ST(1);
    __syncthreads();
    ST(2);
    {
        const u32 c4 = *(const LAS u32*)(smem + L_HIST + hoff);
        u32x4 v = *(const LAS u32x4*)(smem + L_STG + stg) | tg;
        if (__builtin_expect(c4 > 64, 0)) {
            v[0] |= c4 > 128 ? F_OVER : F_MORE;
            u32x4 v2 = *(const LAS u32x4*)(smem + L_STG + stg + 64);
#pragma unroll
            for (int j = 0; j < 4; ++j) v2[j] = (64 + k16 + j * 4 < c4 ? v2[j] : NONE) | tg[j];
            __builtin_amdgcn_raw_buffer_store_b128(v2, rw, poff + (WS_B - WS_A), pso, 16);
        }
        __builtin_amdgcn_raw_buffer_store_b128(v, rw, poff, pso, 16);
    }
    ST(3);
    if (w >= 8) {
        if (XPF == 2) xtouch();
        if (XPF == 1 || XPF == 2) {
            asm volatile("s_waitcnt vmcnt(2)");
            PIN("+v"(wr[0]), "+v"(wr[1]), "+v"(wr[2]), "+v"(wr[3]), "+v"(xr0), "+v"(xr1));
        } else
        asm volatile("s_waitcnt vmcnt(1)"
                     : "+v"(wr[0]), "+v"(wr[1]), "+v"(wr[2]), "+v"(wr[3]), "+v"(xr0), "+v"(xr1));
        LAS char* p = smem + (L_WQ - 8 * 4096) + (w * 4096 + (t16 & 0x3f0));
#pragma unroll
        for (int j = 0; j < 4; ++j) *(LAS u32x4*)(p + j * 1024) = wr[j];
        p = smem + (L_XS - 8 * 2048) + (w * 2048 + (t16 & 0x3f0));
        *(LAS u32x4*)p = xr0; *(LAS u32x4*)(p + 1024) = xr1;
    } else {
        *(LAS u32x4*)(smem + L_LIST + t16) = none; *(LAS u32x4*)(smem + L_LIST + 8192 + t16) = none;
        asm volatile("s_waitcnt vmcnt(1)" : "+v"(pf), "+v"(tch));
    }
    __syncthreads();
    ST(6);
    {
        WFrag f; f32x4 o0, o1; bf16x8 bf[4];
#pragma unroll
        for (int s = 0; s < 4; ++s) bf[s] = *(const LAS bf16x8*)(smem + (xsp ^ (s << 6)));
        wfrag(smem + wq0, smem + wq1, f);
        wgemm(f, bf, o0, o1);
        const float sc = -0.08838834764831845f * 1.4426950408889634f;
        o0 *= sc; o1 *= sc;
        *(LAS u32x2*)(smem + qw0) = (u32x2){pk_bf16(o0[0], o0[1]), pk_bf16(o0[2], o0[3])};
        *(LAS u32x2*)(smem + qw1) = (u32x2){pk_bf16(o1[0], o1[1]), pk_bf16(o1[2], o1[3])};
    }
    ST(4);
    if (XPF == 3 && w >= 8) xtouch();
    const u32x4 first = load_piece(rw, t16, WS_A + b * (NB * 64));
    __builtin_amdgcn_sched_barrier(0);
    u32x4 mk;
    {
        const uint64_t m = 0xffffull << (t16 & 48);
        const u32 up = __builtin_amdgcn_sbfe(tid, 2, 1);
        mk[0] = (u32)m & ~up; mk[1] = (u32)(m >> 32) & ~up; mk[2] = (u32)m & up; mk[3] = (u32)(m >> 32) & up;
    }
    u32 loff = t16 & 0x70, r4 = t2 & 12, h4 = (tid >> 1) & 4, emask = EMASK, nmask = NMASK;
    u32 slb = h4 * (SLOT_S / 4) + ((tid & 16) << 4) - loff + (L_SLOT + w * 2 * SLOT_S);
    u32 agw = t16 + (tid & 0x3f0), ooff = (c16 << 12) | g16, l4 = lane * 4, sgi = t2 & 0xfc;
    u32 sub = ~tid & 32, lrow = lane * ((ROW_S << 16) + 256) + sub;
    u32 sla = (h4 << 6) + r4 + (L_LIST + w * 512), srow = h4 * (ROW_S / 4) + loff + w * 2 * ROW_S;
    u32 vva = ((c16 ^ (g16 << 2)) ^ ((w & 1) << 4)) + (g16 << 4) + w * 2048, vvp = (t16 & 0x3f0) + (L_WV + w * 2048);
    PIN("+v"(mk), "+v"(loff), "+v"(r4), "+v"(h4), "+v"(emask), "+v"(nmask), "+v"(slb));
    PIN("+v"(agw), "+v"(ooff), "+v"(l4), "+v"(lrow), "+v"(sgi), "+v"(sub), "+v"(sla), "+v"(srow), "+v"(vva), "+v"(vvp));
    const auto rx = __builtin_amdgcn_make_buffer_rsrc((void*)x, 0, N * 256, 0x00020000);
    auto append = [&](const u32x4 e) {
        u32 r[4], n4[4];
#pragma unroll
        for (int j = 0; j < 4; ++j) {
            n4[j] = e[j] & nmask;
            asm("" : "=v"(r[j]));
            if (n4[j] != NMASK) r[j] = lds_add((LAS u32*)(smem + L_CNT + n4[j]), four);
        }
#pragma unroll
        for (int j = 0; j < 4; ++j) *(LAS u32*)(smem + L_LIST + and_or(r[j], nmask, n4[j] << 6)) = e[j];
    };
    u32x4 vr0, vr1;
    {
        u32x4 ea = poll_piece(first, rw, t16, WS_A + b * (NB * 64), tg);
        PIN("+v"(ea), "+v"(xp0), "+v"(xp1));
        ST(5);
        asm volatile("buffer_load_dwordx4 %0, %2, %3, 0 offen\n\tbuffer_load_dwordx4 %1, %2, %3, 0 offen offset:1024"
                     : "=&v"(vr0), "=&v"(vr1) : "v"(vva), "s"(srd(wv, 32768)));
        append(ea);
        if (__builtin_expect((ea[0] & (F_MORE | F_OVER)) != 0, 0)) {
            if (ea[0] & F_OVER) lds_add((LAS u32*)(smem + L_CNT + NMASK), 4096u);
            else append(poll_piece(load_piece(rw, t16, WS_B + b * (NB * 64)), rw, t16, WS_B + b * (NB * 64), tg));
        }
    }
    __syncthreads();
    __builtin_amdgcn_s_setprio(3);
    ST(7);
    const u32 c4 = *(const LAS u32*)(smem + L_CNT + l4);
    f32x4 tot0 = {0.f, 0.f, 0.f, 0.f}, tot1 = tot0;
    WFrag wf;
    u32 aa, ab, ea, eb, rowa, rowb;
    auto fetch = [&](Quad& A, Quad& B) {
        const u32 oa = and_or(ea, emask, loff), ob = and_or(eb, emask, loff);
        A.x0 = __builtin_amdgcn_raw_buffer_load_b128(rx, oa, 0, 0); A.x1 = __builtin_amdgcn_raw_buffer_load_b128(rx, oa + 128, 0, 0);
        B.x0 = __builtin_amdgcn_raw_buffer_load_b128(rx, ob, 0, 0); B.x1 = __builtin_amdgcn_raw_buffer_load_b128(rx, ob + 128, 0, 0);
        __builtin_amdgcn_sched_barrier(0);
        A.row = rowa; B.row = rowb;
        A.q0 = *(const LAS u32x4*)(smem + L_Q + rowa); A.q1 = *(const LAS u32x4*)(smem + L_Q + 128 + rowa);
        B.q0 = *(const LAS u32x4*)(smem + L_Q + rowb); B.q1 = *(const LAS u32x4*)(smem + L_Q + 128 + rowb);
    };
    Quad A0, B0, A1, B1;
    ea = *(const LAS u32*)(smem + sla); eb = *(const LAS u32*)(smem + 16 + sla);
    rowa = rowb = srow;
    fetch(A0, B0);
    ST(8);
    asm volatile("s_waitcnt vmcnt(4)" : "+v"(vr0), "+v"(vr1));
    if (b == 0 && tid == 0) __builtin_amdgcn_raw_buffer_store_b128(tg, rw, 0, 0, 16);
    if (__builtin_expect(__ballot(c4 > 256) == 0, 1)) {
        A0.row = B0.row = FIX0 * ROW_S + loff;
        const u32 nq = (__builtin_elementwise_sub_sat(c4, sub) + 12) >> 4;
        const u32 inc = dpp_scan_incl(nq), exc = inc - nq;
        const int nbt = (__builtin_amdgcn_readlane(inc, 63) + 3) >> 2, na = nbt >> 4, nr = nbt & 15;
        const int nb = na + (w < nr), n = 2 * nb;
        const int q0 = 4 * (na * w + min(w, nr));
        const u32 pk = lrow - exc * 16;
        int mlo = __popcll(__ballot(exc <= (u32)q0)) - 1, mhi = __popcll(__ballot(exc < (u32)(q0 + 2 * n)));
        PIN("+s"(mlo), "+s"(mhi));
        const u32 q = q0 + lane;
        u32 off = __builtin_amdgcn_readlane(pk, mlo);
        for (int m = mlo + 1; m < mhi; ++m) {
            const u32 s = __builtin_amdgcn_readlane(exc, m), p = __builtin_amdgcn_readlane(pk, m);
            off = q >= s ? p : off;
        }
        const u32 sched = min(off + q * 16, 63u * ((ROW_S << 16) + 256) + 240);
        u32 idx = h4 * n;
        auto addr = [&](int o) { aa = __builtin_amdgcn_ds_bpermute(idx + o, sched); ab = __builtin_amdgcn_ds_bpermute(idx + o + 4, sched); };
        auto ents = [&]() {
            ea = *(const LAS u32*)(smem + L_LIST + ((aa & 0xffff) + r4)); eb = *(const LAS u32*)(smem + L_LIST + ((ab & 0xffff) + r4));
            rowa = (aa >> 16) + loff; rowb = (ab >> 16) + loff;
        };
        addr(0); ents(); addr(8);
        fetch(A1, B1);
        ents(); addr(16);
        __builtin_amdgcn_s_setprio(2);
        *(LAS u32x4*)(smem + vvp) = vr0; *(LAS u32x4*)(smem + vvp + 1024) = vr1;
        if (w == 15) {
            const float r1 = __builtin_amdgcn_rcpf((float)(2 * na + 2)), r0 = __builtin_amdgcn_rcpf((float)(2 * na));
            const u32 qs = 4 * nr * (na + 1);
            const float sh = (float)(2 * nr) - (float)qs * r0;
            const float f = (float)exc + 0.5f, l = (float)inc - 0.5f;
            const u32 gf = (u32)(exc < qs ? f * r1 : f * r0 + sh), gl = (u32)(inc <= qs ? l * r1 : l * r0 + sh);
            *(LAS u32*)(smem + L_SEG + l4) = nq > 0 ? (gf * SLOT_S) << 8 | (gl - gf + 1) : 0u;
        }
        asm volatile("" ::: "memory");
        if (lane == 0) lds_add((LAS u32*)(smem + L_ARR), 1u);
        const unsigned long long chg = __ballot((sched >> 16) != (dppu<0x138>(sched) >> 16));
        unsigned long long fl = (((chg | (chg >> n)) & 0xffffffffull) | 1) << 2;
        const bool mine = (lane >> 5) == ((lane >> 3) & 1);
        f32x4 acc0 = {0.f, 0.f, 0.f, 0.f}, acc1 = acc0;
        u32 cur = A0.row;
        auto flush = [&]() { LAS char* p = smem + (slb + cur * 2); *(LAS f32x4*)p = acc0; *(LAS f32x4*)(p + 128) = acc1; };
        auto add = [&](const Quad& A, const u32x4 am, bool check) {
            if (__builtin_expect(check, 0)) {
                asm volatile("");
                if (mine && A.row != cur) { flush(); acc0 = (f32x4){0.f, 0.f, 0.f, 0.f}; acc1 = acc0; }
            }
            cur = A.row;
            acc0 = mfma16(am, A.x0, acc0); acc1 = mfma16(am, A.x1, acc1);
        };
        auto step = [&](const Quad& A, const Quad& B) {
            const u32 a2 = alphas(A, B);
            const u32 up = dppu<0x114>(a2), dn = dppu<0x104>(a2);
            const u32x4 ma = {a2 & mk[0], a2 & mk[1], up & mk[2], up & mk[3]};
            const u32x4 mb = {dn & mk[0], dn & mk[1], a2 & mk[2], a2 & mk[3]};
            add(A, ma, fl & 1); add(B, mb, fl & 2);
            fl >>= 2;
        };
        int t = 1;
        for (; t + 1 < nb; t += 2) {
            step(A0, B0); fetch(A0, B0); ents(); addr(24);
            asm volatile("s_cmp_lt_i32 %0, " PRIO_A "\n\ts_cbranch_scc1 1f\n\ts_cmp_lt_i32 %0, " PRIO_B "\n\ts_setprio 1\n\ts_cbranch_scc1 1f\n\ts_setprio 0\n1:" :: "s"(t) : "scc");
            step(A1, B1); fetch(A1, B1); ents(); addr(32);
            idx += 16; PIN("+v"(idx));
        }
        u32 seg;
        auto tail = [&]() {
            while (__builtin_amdgcn_readfirstlane(*(const volatile LAS u32*)(smem + L_ARR)) < 16) __builtin_amdgcn_s_sleep(1);
            asm volatile("" ::: "memory");
            seg = *(const LAS u32*)(smem + L_SEG + sgi);
            wfrag(smem + wvp + (L_WV - L_WQ), smem + wvp + (L_WV - L_WQ) + 8, wf);
        };
        step(A0, B0);
        if (t < nb) { fetch(A0, B0); step(A1, B1); tail(); step(A0, B0); } else { tail(); step(A1, B1); }
        if (mine) flush();
        ST(9);
        const u32 cmb = L_SLOT + agw * 2 - (l4 & 60) * 4;
        const u32 ns = seg & 255, r0 = cmb + (seg >> 8);
        u32 p0 = w < 8 ? cmb + FIX0 * SLOT_S : L_ZERO, p1 = ns > 0 ? r0 : L_ZERO, p2 = ns > 1 ? r0 + SLOT_S : L_ZERO;
        PIN("+v"(p0), "+v"(p1), "+v"(p2));
        __syncthreads();
        ST(10);
        tot0 = *(const LAS f32x4*)(smem + p0) + *(const LAS f32x4*)(smem + p1) + *(const LAS f32x4*)(smem + p2);
        tot1 = *(const LAS f32x4*)(smem + p0 + 256) + *(const LAS f32x4*)(smem + p1 + 256) + *(const LAS f32x4*)(smem + p2 + 256);
        if (__builtin_expect(ns > 2, 0)) {
            const LAS char* r = smem + r0 + 2 * SLOT_S;
            for (u32 k = ns - 2; k > 0; --k, r += SLOT_S) { tot0 += *(const LAS f32x4*)r; tot1 += *(const LAS f32x4*)(r + 256); }
        }
    } else {
        *(LAS u32x4*)(smem + vvp) = vr0; *(LAS u32x4*)(smem + vvp + 1024) = vr1;
        const int g = lane >> 4, i16 = lane & 15;
        const auto rd = __builtin_amdgcn_make_buffer_rsrc((void*)dst, 0, E * 4, 0x00020000);
        const auto rs = __builtin_amdgcn_make_buffer_rsrc((void*)src, 0, E * 4, 0x00020000);
        const u32x4 qv = *(const LAS u32x4*)(smem + L_Q + (4 * w + g) * ROW_S + i16 * 16);
        for (int base = 0; base < E; base += 4096) {
            const int f0 = base + tid * 4;
            const u32x4 dd = __builtin_amdgcn_raw_buffer_load_b128(rd, f0 * 4, 0, 0), ss = __builtin_amdgcn_raw_buffer_load_b128(rs, f0 * 4, 0, 0);
            u32 e[4];
#pragma unroll
            for (int j = 0; j < 4; ++j) e[j] = (f0 < E && (dd[j] & 255) == (u32)b) ? ((ss[j] << 8) | ((dd[j] >> 6) & NMASK)) : NONE;
            for (;;) {
                __syncthreads();
                if (tid < 64) *(LAS u32*)(smem + L_CNT + tid * 4) = 0;
                __syncthreads();
#pragma unroll 1
                for (int j = 0; j < 4; ++j) {
                    const u32 ej = e[0];
                    u32 keep = ej;
                    if (ej != NONE) {
                        const u32 r = lds_add((LAS u32*)(smem + L_CNT + (ej & NMASK)), 4u);
                        if (r < 256) { *(LAS u32*)(smem + L_LIST + ((ej & NMASK) << 6) + r) = ej; keep = NONE; }
                    }
                    e[0] = e[1]; e[1] = e[2]; e[2] = e[3]; e[3] = keep;
                }
                __syncthreads();
                const u32 call = *(const LAS u32*)(smem + L_CNT + lane * 4);
                const u32 cn = min(*(const LAS u32*)(smem + L_CNT + (4 * w + g) * 4), 256u);
                for (u32 k = 0; k < cn; k += 4) {
                    const u32 ej = *(const LAS u32*)(smem + L_LIST + (4 * w + g) * 256 + k);
                    const u32x4 xv = __builtin_amdgcn_raw_buffer_load_b128(rx, (ej & EMASK) | (i16 * 16), 0, 0);
                    float xf[8], u = 0.f;
#pragma unroll
                    for (int j = 0; j < 4; ++j) {
                        xf[2 * j] = __builtin_bit_cast(float, xv[j] << 16); xf[2 * j + 1] = __builtin_bit_cast(float, xv[j] & 0xffff0000u);
                        u += xf[2 * j] * __builtin_bit_cast(float, qv[j] << 16) + xf[2 * j + 1] * __builtin_bit_cast(float, qv[j] & 0xffff0000u);
                    }
                    u += dppf<0x128>(u); u += dppf<0x124>(u); u += dppf<0x122>(u); u += dppf<0x121>(u);
                    const float al = __builtin_amdgcn_rcpf(1.0f + __builtin_amdgcn_exp2f(u));
#pragma unroll
                    for (int j = 0; j < 4; ++j) { tot0[j] += al * xf[j]; tot1[j] += al * xf[4 + j]; }
                }
                if (__ballot(call > 256) == 0) break;
            }
        }
        __syncthreads();
        wfrag(smem + wvp + (L_WV - L_WQ), smem + wvp + (L_WV - L_WQ) + 8, wf);
    }
    *(LAS u32x4*)(smem + L_AGG + agw) = (u32x4){pk_bf16(tot0[0], tot0[1]), pk_bf16(tot0[2], tot0[3]), pk_bf16(tot1[0], tot1[1]), pk_bf16(tot1[2], tot1[3])};
    __syncthreads();
    {
        f32x4 o0, o1; bf16x8 bf[4];
#pragma unroll
        for (int s = 0; s < 4; ++s) bf[s] = *(const LAS bf16x8*)(smem + agr + s * 64);
        wgemm(wf, bf, o0, o1);
        const u32x4 ov = {pk_bf16(o0[0], o0[1]), pk_bf16(o0[2], o0[3]), pk_bf16(o1[0], o1[1]), pk_bf16(o1[2], o1[3])};
        const auto ro = __builtin_amdgcn_make_buffer_rsrc((void*)out, 0, N * 256, 0x00020000);
        __builtin_amdgcn_raw_buffer_store_b128(ov, ro, ooff, (nt << 20) + b * 256 + cg * 64, OUT_AUX);
    }
    ST(11);
    ST_END(0, 11);
}

extern "C" void kernel_launch(void* const* d_in, const int* in_sizes, int n_in,
                              void* d_out, int out_size, void* d_ws, size_t ws_size,
                              hipStream_t stream) {
    const int N = in_sizes[0] / 128, E = in_sizes[1];
    k_graph<<<NB, 1024, 0, stream>>>((const char*)d_in[0], (const int*)d_in[1], (const int*)d_in[2], (const char*)d_in[3], (const char*)d_in[4],
                                     (char*)d_out, (char*)d_ws, N, E
    );
}
```

```cpp
#define KK 0
#define MG 1
#define MT 64
#define ML 0
#define MV 0
#include <hip/hip_runtime.h>
#include <stdint.h>

typedef uint32_t u32;
typedef __attribute__((ext_vector_type(4))) uint32_t u32x4;
typedef __attribute__((ext_vector_type(2))) uint32_t u32x2;
typedef __attribute__((ext_vector_type(4))) float f32x4;
typedef __attribute__((ext_vector_type(4))) short s16x4;
typedef __attribute__((ext_vector_type(8))) __bf16 bf16x8;
typedef __attribute__((ext_vector_type(2))) __bf16 bf16x2;
#define LAS __attribute__((address_space(3)))

#define ST_DECL
#define ST(i)
#define ST_END(lo, hi)
#define CODEPF 1
#define EDGE_MOD " nt"
#define OUT_AUX 16
#define USEPRIO 1
#define PRIO_A "2"
#define PRIO_B "4"
#define POLL_SLEEP 0
#define XPF 0
#define XPF_N 1

constexpr int CODE_LINES = 56;
constexpr int NB = 256;
constexpr u32 EMASK = 0x003FFF00u, NMASK = 0xFCu;
constexpr u32 NONE = EMASK | NMASK;
constexpr u32 TAG0 = 0xFFC00000u, TAG = 0xFFC00003u, F_MORE = 1u, F_OVER = 2u;
constexpr int WS_A = 4096, WS_B = WS_A + NB * NB * 64, WS_END = WS_B + NB * NB * 64;

constexpr int STG_S = 144;
constexpr int ROW_S = 272, SLOT_S = 2 * ROW_S;
constexpr int L_Q = 0, L_HIST = L_Q, L_AGG = L_Q, L_LIST = 64 * ROW_S, L_CNT = L_LIST + 16384, L_SEG = L_CNT + 256, L_ARR = L_SEG + 256, L_ZERO = L_ARR + 16, L_SLOT = L_ARR + 512;
constexpr int L_STG = L_SLOT, L_XS = L_SLOT + 256 * STG_S, L_WQ = L_XS + 16384, L_WV = L_WQ + 32768, L_END = L_WV + 32768;
constexpr int FIX0 = 95;
static_assert(L_XS % 256 == 0 && L_WQ % 256 == 0, "addresses are formed with xor");
static_assert(L_SLOT + (FIX0 + 32) * SLOT_S <= L_WV, "slots");

static __device__ __forceinline__ u32 dpp_scan_incl(u32 v) {
    v += __builtin_amdgcn_update_dpp(0u, v, 0x111, 0xf, 0xf, false);
    v += __builtin_amdgcn_update_dpp(0u, v, 0x112, 0xf, 0xf, false);
    v += __builtin_amdgcn_update_dpp(0u, v, 0x114, 0xf, 0xf, false);
    v += __builtin_amdgcn_update_dpp(0u, v, 0x118, 0xf, 0xf, false);
    v += __builtin_amdgcn_update_dpp(0u, v, 0x142, 0xa, 0xf, false);
    v += __builtin_amdgcn_update_dpp(0u, v, 0x143, 0xc, 0xf, false);
    return v;
}
static __device__ __forceinline__ u32 pk_bf16(float a, float b) {
    bf16x2 v = {(__bf16)a, (__bf16)b};
    return __builtin_bit_cast(u32, v);
}
static __device__ __forceinline__ u32 lds_add(LAS u32* p, u32 v) { return __hip_atomic_fetch_add(p, v, __ATOMIC_RELAXED, __HIP_MEMORY_SCOPE_WORKGROUP); }
static __device__ __forceinline__ f32x4 mfma444(u32x2 a, u32x2 b, f32x4 c) {
    return __builtin_amdgcn_mfma_f32_4x4x4bf16_1k(__builtin_bit_cast(s16x4, a), __builtin_bit_cast(s16x4, b), c, 0, 0, 0);
}
template <int CTRL> static __device__ __forceinline__ float dppf(float v) {
    return __builtin_bit_cast(float, __builtin_amdgcn_update_dpp(0u, __builtin_bit_cast(u32, v), CTRL, 0xf, 0xf, true));
}
template <int CTRL> static __device__ __forceinline__ u32 dppu(u32 v) {
    return __builtin_amdgcn_update_dpp(0u, v, CTRL, 0xf, 0xf, true);
}
static __device__ __forceinline__ float diag4(f32x4 d) {
    float t = d[0] + dppf<0x55>(d[1]);
    t += dppf<0xAA>(d[2]);
    t += dppf<0xFF>(d[3]);
    return t;
}
struct Quad { u32x4 x0, x1, q0, q1; u32 row; };
static __device__ __forceinline__ f32x4 mfma16(u32x4 a, u32x4 b, f32x4 c) {
    return __builtin_amdgcn_mfma_f32_16x16x32_bf16(__builtin_bit_cast(bf16x8, a), __builtin_bit_cast(bf16x8, b), c, 0, 0, 0);
}
static __device__ __forceinline__ u32 alphas(const Quad& a, const Quad& b) {
    const f32x4 z = {0.f, 0.f, 0.f, 0.f};
    f32x4 da = mfma444(a.x0.xy, a.q0.xy, z), db = mfma444(b.x0.xy, b.q0.xy, z);
    da = mfma444(a.x0.zw, a.q0.zw, da); db = mfma444(b.x0.zw, b.q0.zw, db);
    da = mfma444(a.x1.xy, a.q1.xy, da); db = mfma444(b.x1.xy, b.q1.xy, db);
    da = mfma444(a.x1.zw, a.q1.zw, da); db = mfma444(b.x1.zw, b.q1.zw, db);
    const float ta = diag4(da), tb = diag4(db);
    float u;
    asm("s_nop 1\n\t"
        "v_add_f32_dpp %0, %1, %1 row_shl:4 row_mask:0xf bank_mask:0x5\n\t"
        "v_add_f32_dpp %0, %2, %2 row_shr:4 row_mask:0xf bank_mask:0xa\n\t"
        "s_nop 1\n\t"
        "v_exp_f32_dpp %0, %0 quad_perm:[0,0,0,0] row_mask:0xf bank_mask:0xf\n\t"
        "s_nop 0"
        : "=&v"(u) : "v"(ta), "v"(tb));
    const float al = __builtin_amdgcn_rcpf(1.0f + u);
    return pk_bf16(al, al);
}

struct WFrag { bf16x8 a0[4], a1[4]; };
static __device__ __forceinline__ void wfrag(const LAS char* p0, const LAS char* p1, WFrag& f) {
#pragma unroll
    for (int s = 0; s < 4; ++s) {
        const s16x4 a00 = __builtin_amdgcn_ds_read_tr16_b64_v4i16((LAS s16x4*)(p0 + s * 8192));
        const s16x4 a01 = __builtin_amdgcn_ds_read_tr16_b64_v4i16((LAS s16x4*)(p0 + s * 8192 + 1024));
        const s16x4 a10 = __builtin_amdgcn_ds_read_tr16_b64_v4i16((LAS s16x4*)(p1 + s * 8192));
        const s16x4 a11 = __builtin_amdgcn_ds_read_tr16_b64_v4i16((LAS s16x4*)(p1 + s * 8192 + 1024));
        f.a0[s] = __builtin_bit_cast(bf16x8, __builtin_shufflevector(a00, a01, 0, 1, 2, 3, 4, 5, 6, 7));
        f.a1[s] = __builtin_bit_cast(bf16x8, __builtin_shufflevector(a10, a11, 0, 1, 2, 3, 4, 5, 6, 7));
    }
}
static __device__ __forceinline__ void wgemm(const WFrag& f, const bf16x8 (&bf)[4], f32x4& o0, f32x4& o1) {
    o0 = (f32x4){0.f, 0.f, 0.f, 0.f}; o1 = o0;
#pragma unroll
    for (int s = 0; s < 4; ++s) {
        o0 = __builtin_amdgcn_mfma_f32_16x16x32_bf16(f.a0[s], bf[s], o0, 0, 0, 0);
        o1 = __builtin_amdgcn_mfma_f32_16x16x32_bf16(f.a1[s], bf[s], o1, 0, 0, 0);
    }
}
static __device__ __forceinline__ u32x4 srd(const void* p, u32 bytes) {
    const uint64_t a = (uint64_t)p;
    return (u32x4){(u32)a, (u32)(a >> 32) & 0xffffu, bytes, 0x00020000u};
}
static __device__ __forceinline__ void load_w(u32x4 (&v)[4], u32x4 r, u32 va, u32 vb) {
    asm volatile("buffer_load_dwordx4 %0, %4, %6, 0 offen\n\tbuffer_load_dwordx4 %1, %4, %6, 0 offen offset:1024\n\t"
                 "buffer_load_dwordx4 %2, %5, %6, 0 offen offset:2048\n\tbuffer_load_dwordx4 %3, %5, %6, 0 offen offset:3072"
                 : "=&v"(v[0]), "=&v"(v[1]), "=&v"(v[2]), "=&v"(v[3]) : "v"(va), "v"(vb), "s"(r));
}

static __device__ __forceinline__ u32x4 load_piece(__amdgpu_buffer_rsrc_t rw, u32 voff, u32 soff) {
    asm volatile("" ::: "memory");
    return __builtin_amdgcn_raw_buffer_load_b128(rw, voff, soff, 16);
}
static __device__ __forceinline__ u32x4 poll_piece(u32x4 v, __amdgpu_buffer_rsrc_t rw, u32 voff, u32 soff, u32x4 tg) {
    for (int spin = 0; spin < (1 << 20); ++spin) {
        const u32 bad = ((v[0] ^ tg[0]) & TAG0) | (((v[1] ^ tg[1]) | (v[2] ^ tg[2]) | (v[3] ^ tg[3])) & TAG);
        if (bad == 0) break;
        if (POLL_SLEEP) __builtin_amdgcn_s_sleep(POLL_SLEEP);
        v = load_piece(rw, voff, soff);
    }
    return v;
}
static __device__ __forceinline__ u32 and_or(u32 a, u32 m, u32 c) { return __builtin_amdgcn_bitop3_b32(a, m, c, 0xEA); }
#define PIN(...) asm volatile("" : __VA_ARGS__)

__global__ __launch_bounds__(1024) void k_graph(const char* __restrict__ x, const int* __restrict__ src, const int* __restrict__ dst,
                                                const char* __restrict__ wq, const char* __restrict__ wv,
                                                char* __restrict__ out, char* ws, int N, int E
) {
    __shared__ __attribute__((aligned(16))) char smem_[L_END];
    LAS char* smem = (LAS char*)smem_;
    const int tid = threadIdx.x, lane = tid & 63;
    const int w = __builtin_amdgcn_readfirstlane(tid >> 6);
    const int b = blockIdx.x;
    u32 pf = 0;
    if (w == 7) {
        uint64_t pc; asm volatile("s_getpc_b64 %0" : "=s"(pc));
        asm volatile("global_load_dword %0, %1, %2" : "=v"(pf) : "v"(min(lane, CODE_LINES - 1) * 128), "s"(pc));
    }
    asm volatile("" :: "s"(x), "s"(src), "s"(dst), "s"(wq), "s"(wv), "s"(out), "s"(ws), "s"(N), "s"(E));
    ST_DECL;
    ST(0);
    const int CH = ((E + NB * 4 - 1) >> 10) * 4;
    const auto rw = __builtin_amdgcn_make_buffer_rsrc((void*)ws, 0, WS_END, 0x00020000);

    const u32x4 none = {NONE, NONE, NONE, NONE};
    const u32 t16 = tid * 16;
    const int ne = min(CH, E - b * CH);
    const bool binner = w * 256 < ne;
    u32x4 d4, s4;
    if (binner)
        asm volatile("buffer_load_dwordx4 %0, %2, %3, 0 offen" EDGE_MOD "\n\tbuffer_load_dwordx4 %1, %2, %4, 0 offen" EDGE_MOD
                     : "=&v"(d4), "=&v"(s4) : "v"(t16), "s"(srd(dst + b * CH, ne * 4)), "s"(srd(src + b * CH, ne * 4)));
    const u32x4 old = *(const __attribute__((address_space(4))) u32x4*)ws;
    const u32 t2 = tid >> 2, k16 = t16 & 48;
    u32 stg = t2 * STG_S + k16, poff = (t2 << 14) | k16, hoff = tid & 0x3fc, four = 4;
    *(LAS u32x4*)(smem + L_STG + stg) = none;
    if (w < 4) { *(LAS u32*)(smem + L_HIST + tid * 4) = 0; if (w == 0) { *(LAS u32*)(smem + L_CNT + tid * 4) = 0; *(LAS u32*)(smem + L_ARR + tid * 4) = 0; *(LAS u32*)(smem + L_ARR + 256 + (tid & 15) * 4) = 0; } }
    __syncthreads();
    const u32 c16 = t16 & 0xf0, g16 = tid & 48;
    u32x4 wr[4], xr0, xr1;
    if (w >= 8) {
        const int v = w - 8;
        const u32 wva = (c16 ^ (g16 << 2)) + (g16 << 4) + v * 4096;
        load_w(wr, srd(wq, 32768), wva, wva ^ 16);
        const u32 xa = ((c16 ^ g16) ^ ((v & 1) << 7)) + (g16 << 12) + (v << 19);
        if (v * 2048 + b < N)
            asm volatile("buffer_load_dwordx4 %0, %2, %4, 0 offen\n\tbuffer_load_dwordx4 %1, %3, %4, 0 offen"
                         : "=&v"(xr0), "=&v"(xr1) : "v"(xa), "v"((xa ^ 64) + (4 << 16)), "s"(srd(x + b * 256, (N - b) * 256)));
    }
    u32 xp0 = 0, xp1 = 0;
    auto xtouch = [&]() {
        const int v = w - 8, b2 = (b & ~7) | ((b + 1 + (v >> 1)) & 7);
        asm volatile("buffer_load_dword %0, %1, %2, 0 offen" : "=v"(xp0) : "v"(v < 2 * XPF_N ? lane << 16 : -4), "s"(srd(x + b2 * 256 + (v & 1) * 128, (N - b2) * 256 - 128)));
    };
    if (XPF == 1 && w >= 8) xtouch();
    u32x4 tg;
    {
        const uint64_t c0 = (((uint64_t)(old[0] | ~TAG0) << 32) | (old[1] | ~TAG)) + 1, c1 = (((uint64_t)(old[2] | ~TAG) << 32) | (old[3] | ~TAG)) + 1;
        tg[0] = (u32)(c0 >> 32) & TAG0; tg[1] = (u32)c0 & TAG; tg[2] = (u32)(c1 >> 32) & TAG; tg[3] = (u32)c1 & TAG;
    }
    const int nt = w & 3, cg = w >> 2;
    u32 wvp = ((c16 ^ (tid & 16) ^ (cg << 6)) | ((tid & 12) << 6) | (g16 << 7)) + L_WQ;
    u32 wq0 = wvp | ((tid & 1) << 3), wq1 = wq0 ^ 8;
    u32 xsp = ((c16 << 4) | (c16 ^ g16)) + (L_XS + nt * 4096);
    u32 agr = (lane & 15) * ROW_S + g16 + (L_Q + nt * 16 * ROW_S);
    u32 qw0 = agr + ((tid & 16) >> 1) + cg * 64, qw1 = qw0 ^ 8;
    u32 pso = WS_A + (((b & 7) << 5) | (b >> 3)) * 64;
    PIN("+s"(tg), "+s"(pso), "+v"(poff), "+v"(hoff), "+v"(stg), "+v"(four), "+v"(wvp), "+v"(wq0), "+v"(wq1), "+v"(xsp), "+v"(qw0), "+v"(qw1), "+v"(agr));
    if (binner) {
        asm volatile("s_waitcnt vmcnt(0)" : "+v"(d4), "+v"(s4));
        if (tid * 4 < ne) {
            u32 rank[4];
#pragma unroll
            for (int j = 0; j < 4; ++j) rank[j] = lds_add((LAS u32*)(smem + L_HIST + (d4[j] & 255) * 4), four);
#pragma unroll
            for (int j = 0; j < 4; ++j)
                *(LAS u32*)(smem + L_STG + (d4[j] & 255) * STG_S + min(rank[j], 128u)) = (s4[j] << 8) | ((d4[j] >> 6) & NMASK);
        }
    }
    ST(1);
    __syncthreads();
    ST(2);
    {
        const u32 c4 = *(const LAS u32*)(smem + L_HIST + hoff);
        u32x4 v = *(const LAS u32x4*)(smem + L_STG + stg) | tg;
        if (__builtin_expect(c4 > 64, 0)) {
            v[0] |= c4 > 128 ? F_OVER : F_MORE;
            u32x4 v2 = *(const LAS u32x4*)(smem + L_STG + stg + 64);
#pragma unroll
            for (int j = 0; j < 4; ++j) v2[j] = (64 + k16 + j * 4 < c4 ? v2[j] : NONE) | tg[j];
            __builtin_amdgcn_raw_buffer_store_b128(v2, rw, poff + (WS_B - WS_A), pso, 16);
        }
        __builtin_amdgcn_raw_buffer_store_b128(v, rw, poff, pso, 16);
    }
    ST(3);
    if (w >= 8) {
        if (XPF == 2) xtouch();
        if (XPF == 1 || XPF == 2) {
            asm volatile("s_waitcnt vmcnt(2)");
            PIN("+v"(wr[0]), "+v"(wr[1]), "+v"(wr[2]), "+v"(wr[3]), "+v"(xr0), "+v"(xr1));
        } else
        asm volatile("s_waitcnt vmcnt(1)"
                     : "+v"(wr[0]), "+v"(wr[1]), "+v"(wr[2]), "+v"(wr[3]), "+v"(xr0), "+v"(xr1));
        LAS char* p = smem + (L_WQ - 8 * 4096) + (w * 4096 + (t16 & 0x3f0));
#pragma unroll
        for (int j = 0; j < 4; ++j) *(LAS u32x4*)(p + j * 1024) = wr[j];
        p = smem + (L_XS - 8 * 2048) + (w * 2048 + (t16 & 0x3f0));
        *(LAS u32x4*)p = xr0; *(LAS u32x4*)(p + 1024) = xr1;
    } else {
        *(LAS u32x4*)(smem + L_LIST + t16) = none; *(LAS u32x4*)(smem + L_LIST + 8192 + t16) = none;
        asm volatile("s_waitcnt vmcnt(1)" : "+v"(pf));
    }
    __syncthreads();
    ST(6);
    {
        WFrag f; f32x4 o0, o1; bf16x8 bf[4];
#pragma unroll
        for (int s = 0; s < 4; ++s) bf[s] = *(const LAS bf16x8*)(smem + (xsp ^ (s << 6)));
        wfrag(smem + wq0, smem + wq1, f);
        wgemm(f, bf, o0, o1);
        const float sc = -0.08838834764831845f * 1.4426950408889634f;
        o0 *= sc; o1 *= sc;
        *(LAS u32x2*)(smem + qw0) = (u32x2){pk_bf16(o0[0], o0[1]), pk_bf16(o0[2], o0[3])};
        *(LAS u32x2*)(smem + qw1) = (u32x2){pk_bf16(o1[0], o1[1]), pk_bf16(o1[2], o1[3])};
    }
    ST(4);
    if (XPF == 3 && w >= 8) xtouch();
    const u32x4 first = load_piece(rw, t16, WS_A + b * (NB * 64));
    __builtin_amdgcn_sched_barrier(0);
    u32x4 mk;
    {
        const uint64_t m = 0xffffull << (t16 & 48);
        const u32 up = __builtin_amdgcn_sbfe(tid, 2, 1);
        mk[0] = (u32)m & ~up; mk[1] = (u32)(m >> 32) & ~up; mk[2] = (u32)m & up; mk[3] = (u32)(m >> 32) & up;
    }
    u32 loff = t16 & 0x70, r4 = t2 & 12, h4 = (tid >> 1) & 4, emask = EMASK, nmask = NMASK;
    u32 slb = h4 * (SLOT_S / 4) + ((tid & 16) << 4) - loff + (L_SLOT + w * 2 * SLOT_S);
    u32 agw = t16 + (tid & 0x3f0), ooff = (c16 << 12) | g16, l4 = lane * 4, sgi = t2 & 0xfc;
    u32 sub = ~tid & 32, lrow = lane * ((ROW_S << 16) + 256) + sub;
    u32 sla = (h4 << 6) + r4 + (L_LIST + w * 512), srow = h4 * (ROW_S / 4) + loff + w * 2 * ROW_S;
    u32 vva = ((c16 ^ (g16 << 2)) ^ ((w & 1) << 4)) + (g16 << 4) + w * 2048, vvp = (t16 & 0x3f0) + (L_WV + w * 2048);
    PIN("+v"(mk), "+v"(loff), "+v"(r4), "+v"(h4), "+v"(emask), "+v"(nmask), "+v"(slb));
    PIN("+v"(agw), "+v"(ooff), "+v"(l4), "+v"(lrow), "+v"(sgi), "+v"(sub), "+v"(sla), "+v"(srow), "+v"(vva), "+v"(vvp));
    const auto rx = __builtin_amdgcn_make_buffer_rsrc((void*)x, 0, N * 256, 0x00020000);
    auto append = [&](const u32x4 e) {
        u32 r[4], n4[4];
#pragma unroll
        for (int j = 0; j < 4; ++j) {
            n4[j] = e[j] & nmask;
            asm("" : "=v"(r[j]));
            if (n4[j] != NMASK) r[j] = lds_add((LAS u32*)(smem + L_CNT + n4[j]), four);
        }
#pragma unroll
        for (int j = 0; j < 4; ++j) *(LAS u32*)(smem + L_LIST + and_or(r[j], nmask, n4[j] << 6)) = e[j];
    };
    u32x4 vr0, vr1;
    {
        u32x4 ea = poll_piece(first, rw, t16, WS_A + b * (NB * 64), tg);
        PIN("+v"(ea), "+v"(xp0), "+v"(xp1));
        ST(5);
        asm volatile("buffer_load_dwordx4 %0, %2, %3, 0 offen\n\tbuffer_load_dwordx4 %1, %2, %3, 0 offen offset:1024"
                     : "=&v"(vr0), "=&v"(vr1) : "v"(vva), "s"(srd(wv, 32768)));
        append(ea);
        if (__builtin_expect((ea[0] & (F_MORE | F_OVER)) != 0, 0)) {
            if (ea[0] & F_OVER) lds_add((LAS u32*)(smem + L_CNT + NMASK), 4096u);
            else append(poll_piece(load_piece(rw, t16, WS_B + b * (NB * 64)), rw, t16, WS_B + b * (NB * 64), tg));
        }
    }
    __syncthreads();
    __builtin_amdgcn_s_setprio(3);
    ST(7);
    const u32 c4 = *(const LAS u32*)(smem + L_CNT + l4);
    f32x4 tot0 = {0.f, 0.f, 0.f, 0.f}, tot1 = tot0;
    WFrag wf;
    u32 aa, ab, ea, eb, rowa, rowb;
    auto fetch = [&](Quad& A, Quad& B) {
        const u32 oa = and_or(ea, emask, loff), ob = and_or(eb, emask, loff);
        A.x0 = __builtin_amdgcn_raw_buffer_load_b128(rx, oa, 0, 0); A.x1 = __builtin_amdgcn_raw_buffer_load_b128(rx, oa + 128, 0, 0);
        B.x0 = __builtin_amdgcn_raw_buffer_load_b128(rx, ob, 0, 0); B.x1 = __builtin_amdgcn_raw_buffer_load_b128(rx, ob + 128, 0, 0);
        __builtin_amdgcn_sched_barrier(0);
        A.row = rowa; B.row = rowb;
        A.q0 = *(const LAS u32x4*)(smem + L_Q + rowa); A.q1 = *(const LAS u32x4*)(smem + L_Q + 128 + rowa);
        B.q0 = *(const LAS u32x4*)(smem + L_Q + rowb); B.q1 = *(const LAS u32x4*)(smem + L_Q + 128 + rowb);
    };
    Quad A0, B0, A1, B1;
    ea = *(const LAS u32*)(smem + sla); eb = *(const LAS u32*)(smem + 16 + sla);
    rowa = rowb = srow;
    fetch(A0, B0);
    ST(8);
    asm volatile("s_waitcnt vmcnt(4)" : "+v"(vr0), "+v"(vr1));
    if (b == 0 && tid == 0) __builtin_amdgcn_raw_buffer_store_b128(tg, rw, 0, 0, 16);
    if (__builtin_expect(__ballot(c4 > 256) == 0, 1)) {
        A0.row = B0.row = FIX0 * ROW_S + loff;
        const u32 nq = (__builtin_elementwise_sub_sat(c4, sub) + 12) >> 4;
        const u32 inc = dpp_scan_incl(nq), exc = inc - nq;
        const int nbt = (__builtin_amdgcn_readlane(inc, 63) + 3) >> 2, na = nbt >> 4, nr = nbt & 15;
        const int nb = na + (w < nr), n = 2 * nb;
        const int q0 = 4 * (na * w + min(w, nr));
        const u32 pk = lrow - exc * 16;
        int mlo = __popcll(__ballot(exc <= (u32)q0)) - 1, mhi = __popcll(__ballot(exc < (u32)(q0 + 2 * n)));
        PIN("+s"(mlo), "+s"(mhi));
        const u32 q = q0 + lane;
        u32 off = __builtin_amdgcn_readlane(pk, mlo);
        for (int m = mlo + 1; m < mhi; ++m) {
            const u32 s = __builtin_amdgcn_readlane(exc, m), p = __builtin_amdgcn_readlane(pk, m);
            off = q >= s ? p : off;
        }
        const u32 sched = min(off + q * 16, 63u * ((ROW_S << 16) + 256) + 240);
        u32 idx = h4 * n;
        auto addr = [&](int o) { aa = __builtin_amdgcn_ds_bpermute(idx + o, sched); ab = __builtin_amdgcn_ds_bpermute(idx + o + 4, sched); };
        auto ents = [&]() {
            ea = *(const LAS u32*)(smem + L_LIST + ((aa & 0xffff) + r4)); eb = *(const LAS u32*)(smem + L_LIST + ((ab & 0xffff) + r4));
            rowa = (aa >> 16) + loff; rowb = (ab >> 16) + loff;
        };
        addr(0); ents(); addr(8);
        fetch(A1, B1);
        ents(); addr(16);
        __builtin_amdgcn_s_setprio(2);
        *(LAS u32x4*)(smem + vvp) = vr0; *(LAS u32x4*)(smem + vvp + 1024) = vr1;
        if (w == 15) {
            const float r1 = __builtin_amdgcn_rcpf((float)(2 * na + 2)), r0 = __builtin_amdgcn_rcpf((float)(2 * na));
            const u32 qs = 4 * nr * (na + 1);
            const float sh = (float)(2 * nr) - (float)qs * r0;
            const float f = (float)exc + 0.5f, l = (float)inc - 0.5f;
            const u32 gf = (u32)(exc < qs ? f * r1 : f * r0 + sh), gl = (u32)(inc <= qs ? l * r1 : l * r0 + sh);
            *(LAS u32*)(smem + L_SEG + l4) = nq > 0 ? (gf * SLOT_S) << 8 | (gl - gf + 1) : 0u;
        }
        asm volatile("" ::: "memory");
        if (lane == 0) lds_add((LAS u32*)(smem + L_ARR), 1u);
        const unsigned long long chg = __ballot((sched >> 16) != (dppu<0x138>(sched) >> 16));
        unsigned long long fl = (((chg | (chg >> n)) & 0xffffffffull) | 1) << 2;
        const bool mine = (lane >> 5) == ((lane >> 3) & 1);
        f32x4 acc0 = {0.f, 0.f, 0.f, 0.f}, acc1 = acc0;
        u32 cur = A0.row;
        auto flush = [&]() { LAS char* p = smem + (slb + cur * 2); *(LAS f32x4*)p = acc0; *(LAS f32x4*)(p + 128) = acc1; };
        auto add = [&](const Quad& A, const u32x4 am, bool check) {
            if (__builtin_expect(check, 0)) {
                asm volatile("");
                if (mine && A.row != cur) { flush(); acc0 = (f32x4){0.f, 0.f, 0.f, 0.f}; acc1 = acc0; }
            }
            cur = A.row;
            acc0 = mfma16(am, A.x0, acc0); acc1 = mfma16(am, A.x1, acc1);
        };
        auto step = [&](const Quad& A, const Quad& B) {
            const u32 a2 = alphas(A, B);
            const u32 up = dppu<0x114>(a2), dn = dppu<0x104>(a2);
            const u32x4 ma = {a2 & mk[0], a2 & mk[1], up & mk[2], up & mk[3]};
            const u32x4 mb = {dn & mk[0], dn & mk[1], a2 & mk[2], a2 & mk[3]};
            add(A, ma, fl & 1); add(B, mb, fl & 2);
            fl >>= 2;
        };
        int t = 1;
        for (; t + 1 < nb; t += 2) {
            step(A0, B0); fetch(A0, B0); ents(); addr(24);
            asm volatile("s_cmp_lt_i32 %0, " PRIO_A "\n\ts_cbranch_scc1 1f\n\ts_cmp_lt_i32 %0, " PRIO_B "\n\ts_setprio 1\n\ts_cbranch_scc1 1f\n\ts_setprio 0\n1:" :: "s"(t) : "scc");
            step(A1, B1); fetch(A1, B1); ents(); addr(32);
            idx += 16; PIN("+v"(idx));
        }
        u32 seg;
        auto tail = [&]() {
            while (__builtin_amdgcn_readfirstlane(*(const volatile LAS u32*)(smem + L_ARR)) < 16) __builtin_amdgcn_s_sleep(1);
            asm volatile("" ::: "memory");
            seg = *(const LAS u32*)(smem + L_SEG + sgi);
            wfrag(smem + wvp + (L_WV - L_WQ), smem + wvp + (L_WV - L_WQ) + 8, wf);
        };
        step(A0, B0);
        if (t < nb) { fetch(A0, B0); step(A1, B1); tail(); step(A0, B0); } else { tail(); step(A1, B1); }
        if (mine) flush();
        ST(9);
        const u32 cmb = L_SLOT + agw * 2 - (l4 & 60) * 4;
        const u32 ns = seg & 255, r0 = cmb + (seg >> 8);
        u32 p0 = w < 8 ? cmb + FIX0 * SLOT_S : L_ZERO, p1 = ns > 0 ? r0 : L_ZERO, p2 = ns > 1 ? r0 + SLOT_S : L_ZERO;
        PIN("+v"(p0), "+v"(p1), "+v"(p2));
        __syncthreads();
        ST(10);
        tot0 = *(const LAS f32x4*)(smem + p0) + *(const LAS f32x4*)(smem + p1) + *(const LAS f32x4*)(smem + p2);
        tot1 = *(const LAS f32x4*)(smem + p0 + 256) + *(const LAS f32x4*)(smem + p1 + 256) + *(const LAS f32x4*)(smem + p2 + 256);
        if (__builtin_expect(ns > 2, 0)) {
            const LAS char* r = smem + r0 + 2 * SLOT_S;
            for (u32 k = ns - 2; k > 0; --k, r += SLOT_S) { tot0 += *(const LAS f32x4*)r; tot1 += *(const LAS f32x4*)(r + 256); }
        }
    } else {
        *(LAS u32x4*)(smem + vvp) = vr0; *(LAS u32x4*)(smem + vvp + 1024) = vr1;
        const int g = lane >> 4, i16 = lane & 15;
        const auto rd = __builtin_amdgcn_make_buffer_rsrc((void*)dst, 0, E * 4, 0x00020000);
        const auto rs = __builtin_amdgcn_make_buffer_rsrc((void*)src, 0, E * 4, 0x00020000);
        const u32x4 qv = *(const LAS u32x4*)(smem + L_Q + (4 * w + g) * ROW_S + i16 * 16);
        for (int base = 0; base < E; base += 4096) {
            const int f0 = base + tid * 4;
            const u32x4 dd = __builtin_amdgcn_raw_buffer_load_b128(rd, f0 * 4, 0, 0), ss = __builtin_amdgcn_raw_buffer_load_b128(rs, f0 * 4, 0, 0);
            u32 e[4];
#pragma unroll
            for (int j = 0; j < 4; ++j) e[j] = (f0 < E && (dd[j] & 255) == (u32)b) ? ((ss[j] << 8) | ((dd[j] >> 6) & NMASK)) : NONE;
            for (;;) {
                __syncthreads();
                if (tid < 64) *(LAS u32*)(smem + L_CNT + tid * 4) = 0;
                __syncthreads();
#pragma unroll 1
                for (int j = 0; j < 4; ++j) {
                    const u32 ej = e[0];
                    u32 keep = ej;
                    if (ej != NONE) {
                        const u32 r = lds_add((LAS u32*)(smem + L_CNT + (ej & NMASK)), 4u);
                        if (r < 256) { *(LAS u32*)(smem + L_LIST + ((ej & NMASK) << 6) + r) = ej; keep = NONE; }
                    }
                    e[0] = e[1]; e[1] = e[2]; e[2] = e[3]; e[3] = keep;
                }
                __syncthreads();
                const u32 call = *(const LAS u32*)(smem + L_CNT + lane * 4);
                const u32 cn = min(*(const LAS u32*)(smem + L_CNT + (4 * w + g) * 4), 256u);
                for (u32 k = 0; k < cn; k += 4) {
                    const u32 ej = *(const LAS u32*)(smem + L_LIST + (4 * w + g) * 256 + k);
                    const u32x4 xv = __builtin_amdgcn_raw_buffer_load_b128(rx, (ej & EMASK) | (i16 * 16), 0, 0);
                    float xf[8], u = 0.f;
#pragma unroll
                    for (int j = 0; j < 4; ++j) {
                        xf[2 * j] = __builtin_bit_cast(float, xv[j] << 16); xf[2 * j + 1] = __builtin_bit_cast(float, xv[j] & 0xffff0000u);
                        u += xf[2 * j] * __builtin_bit_cast(float, qv[j] << 16) + xf[2 * j + 1] * __builtin_bit_cast(float, qv[j] & 0xffff0000u);
                    }
                    u += dppf<0x128>(u); u += dppf<0x124>(u); u += dppf<0x122>(u); u += dppf<0x121>(u);
                    const float al = __builtin_amdgcn_rcpf(1.0f + __builtin_amdgcn_exp2f(u));
#pragma unroll
                    for (int j = 0; j < 4; ++j) { tot0[j] += al * xf[j]; tot1[j] += al * xf[4 + j]; }
                }
                if (__ballot(call > 256) == 0) break;
            }
        }
        __syncthreads();
        wfrag(smem + wvp + (L_WV - L_WQ), smem + wvp + (L_WV - L_WQ) + 8, wf);
    }
    *(LAS u32x4*)(smem + L_AGG + agw) = (u32x4){pk_bf16(tot0[0], tot0[1]), pk_bf16(tot0[2], tot0[3]), pk_bf16(tot1[0], tot1[1]), pk_bf16(tot1[2], tot1[3])};
    __syncthreads();
    {
        f32x4 o0, o1; bf16x8 bf[4];
#pragma unroll
        for (int s = 0; s < 4; ++s) bf[s] = *(const LAS bf16x8*)(smem + agr + s * 64);
        wgemm(wf, bf, o0, o1);
        const u32x4 ov = {pk_bf16(o0[0], o0[1]), pk_bf16(o0[2], o0[3]), pk_bf16(o1[0], o1[1]), pk_bf16(o1[2], o1[3])};
        const auto ro = __builtin_amdgcn_make_buffer_rsrc((void*)out, 0, N * 256, 0x00020000);
        __builtin_amdgcn_raw_buffer_store_b128(ov, ro, ooff, (nt << 20) + b * 256 + cg * 64, OUT_AUX);
    }
    ST(11);
    if (blockIdx.x == 0 && threadIdx.x == 0) {
        const unsigned long long* p = (const unsigned long long*)(ws + (32 << 20));
        unsigned long long d = p[16] - p[0]; if (d > 3000) d = 0;
        const unsigned long long t1 = wall_clock64();
        while (wall_clock64() - t1 < 300 + KK * d) __builtin_amdgcn_s_sleep(1);
    }
}
__global__ void k_head(char* ws) { if (threadIdx.x == 0) *(unsigned long long*)(ws + (32 << 20)) = wall_clock64(); }
__global__ __launch_bounds__(MT) void k_mid(char* ws, int z) {
    const unsigned long long t = wall_clock64();
    __shared__ int sm[ML / 4 + 1];
    if (z) { sm[threadIdx.x * z] = z; __syncthreads(); ((int*)ws)[threadIdx.x] = sm[(threadIdx.x ^ 1) * z]; }
    if (blockIdx.x == 0 && threadIdx.x == 0) *((unsigned long long*)(ws + (32 << 20)) + 16) = t;
}

extern "C" void kernel_launch(void* const* d_in, const int* in_sizes, int n_in,
                              void* d_out, int out_size, void* d_ws, size_t ws_size,
                              hipStream_t stream) {
    const int N = in_sizes[0] / 128, E = in_sizes[1];
    k_head<<<1, 64, 0, stream>>>((char*)d_ws);
    k_mid<<<MG, MT, 0, stream>>>((char*)d_ws, 0);
    k_graph<<<NB, 1024, 0, stream>>>((const char*)d_in[0], (const int*)d_in[1], (const int*)d_in[2], (const char*)d_in[3], (const char*)d_in[4],
                                     (char*)d_out, (char*)d_ws, N, E
    );
}
```

```cpp
#include <hip/hip_runtime.h>
#include <stdint.h>

typedef uint32_t u32;
typedef __attribute__((ext_vector_type(4))) uint32_t u32x4;
typedef __attribute__((ext_vector_type(2))) uint32_t u32x2;
typedef __attribute__((ext_vector_type(4))) float f32x4;
typedef __attribute__((ext_vector_type(4))) short s16x4;
typedef __attribute__((ext_vector_type(8))) __bf16 bf16x8;
typedef __attribute__((ext_vector_type(2))) __bf16 bf16x2;
#define LAS __attribute__((address_space(3)))

#define ST_DECL
#define ST(i)
#define ST_END(lo, hi)
#define CODEPF 1
#define EDGE_MOD " nt"
#define OUT_AUX 16
#define USEPRIO 1
#define PRIO_A "2"
#define PRIO_B "4"
#define POLL_SLEEP 0
#define XPF 0
#define XPF_N 1

constexpr int CODE_LINES = 56;
constexpr int NB = 256;
constexpr u32 EMASK = 0x003FFF00u, NMASK = 0xFCu;
constexpr u32 NONE = EMASK | NMASK;
constexpr u32 TAG0 = 0xFFC00000u, TAG = 0xFFC00003u, F_MORE = 1u, F_OVER = 2u;
constexpr int WS_A = 4096, WS_B = WS_A + NB * NB * 64, WS_END = WS_B + NB * NB * 64;

constexpr int STG_S = 144;
constexpr int ROW_S = 272, SLOT_S = 2 * ROW_S;
constexpr int L_Q = 0, L_HIST = L_Q, L_AGG = L_Q, L_LIST = 64 * ROW_S, L_CNT = L_LIST + 16384, L_SEG = L_CNT + 256, L_ARR = L_SEG + 256, L_ZERO = L_ARR + 16, L_SLOT = L_ARR + 512;
constexpr int L_STG = L_SLOT, L_XS = L_SLOT + 256 * STG_S, L_WQ = L_XS + 16384, L_WV = L_WQ + 32768, L_END = L_WV + 32768;
constexpr int FIX0 = 95;
static_assert(L_XS % 256 == 0 && L_WQ % 256 == 0, "addresses are formed with xor");
static_assert(L_SLOT + (FIX0 + 32) * SLOT_S <= L_WV, "slots");

static __device__ __forceinline__ u32 dpp_scan_incl(u32 v) {
    v += __builtin_amdgcn_update_dpp(0u, v, 0x111, 0xf, 0xf, false);
    v += __builtin_amdgcn_update_dpp(0u, v, 0x112, 0xf, 0xf, false);
    v += __builtin_amdgcn_update_dpp(0u, v, 0x114, 0xf, 0xf, false);
    v += __builtin_amdgcn_update_dpp(0u, v, 0x118, 0xf, 0xf, false);
    v += __builtin_amdgcn_update_dpp(0u, v, 0x142, 0xa, 0xf, false);
    v += __builtin_amdgcn_update_dpp(0u, v, 0x143, 0xc, 0xf, false);
    return v;
}
static __device__ __forceinline__ u32 pk_bf16(float a, float b) {
    bf16x2 v = {(__bf16)a, (__bf16)b};
    return __builtin_bit_cast(u32, v);
}
static __device__ __forceinline__ u32 lds_add(LAS u32* p, u32 v) { return __hip_atomic_fetch_add(p, v, __ATOMIC_RELAXED, __HIP_MEMORY_SCOPE_WORKGROUP); }
static __device__ __forceinline__ f32x4 mfma444(u32x2 a, u32x2 b, f32x4 c) {
    return __builtin_amdgcn_mfma_f32_4x4x4bf16_1k(__builtin_bit_cast(s16x4, a), __builtin_bit_cast(s16x4, b), c, 0, 0, 0);
}
template <int CTRL> static __device__ __forceinline__ float dppf(float v) {
    return __builtin_bit_cast(float, __builtin_amdgcn_update_dpp(0u, __builtin_bit_cast(u32, v), CTRL, 0xf, 0xf, true));
}
template <int CTRL> static __device__ __forceinline__ u32 dppu(u32 v) {
    return __builtin_amdgcn_update_dpp(0u, v, CTRL, 0xf, 0xf, true);
}
static __device__ __forceinline__ float diag4(f32x4 d) {
    float t = d[0] + dppf<0x55>(d[1]);
    t += dppf<0xAA>(d[2]);
    t += dppf<0xFF>(d[3]);
    return t;
}
struct Quad { u32x4 x0, x1, q0, q1; u32 row; };
static __device__ __forceinline__ f32x4 mfma16(u32x4 a, u32x4 b, f32x4 c) {
    return __builtin_amdgcn_mfma_f32_16x16x32_bf16(__builtin_bit_cast(bf16x8, a), __builtin_bit_cast(bf16x8, b), c, 0, 0, 0);
}
static __device__ __forceinline__ u32 alphas(const Quad& a, const Quad& b) {
    const f32x4 z = {0.f, 0.f, 0.f, 0.f};
    f32x4 da = mfma444(a.x0.xy, a.q0.xy, z), db = mfma444(b.x0.xy, b.q0.xy, z);
    da = mfma444(a.x0.zw, a.q0.zw, da); db = mfma444(b.x0.zw, b.q0.zw, db);
    da = mfma444(a.x1.xy, a.q1.xy, da); db = mfma444(b.x1.xy, b.q1.xy, db);
    da = mfma444(a.x1.zw, a.q1.zw, da); db = mfma444(b.x1.zw, b.q1.zw, db);
    const float ta = diag4(da), tb = diag4(db);
    float u;
    asm("s_nop 1\n\t"
        "v_add_f32_dpp %0, %1, %1 row_shl:4 row_mask:0xf bank_mask:0x5\n\t"
        "v_add_f32_dpp %0, %2, %2 row_shr:4 row_mask:0xf bank_mask:0xa\n\t"
        "s_nop 1\n\t"
        "v_exp_f32_dpp %0, %0 quad_perm:[0,0,0,0] row_mask:0xf bank_mask:0xf\n\t"
        "s_nop 0"
        : "=&v"(u) : "v"(ta), "v"(tb));
    const float al = __builtin_amdgcn_rcpf(1.0f + u);
    return pk_bf16(al, al);
}

struct WFrag { bf16x8 a0[4], a1[4]; };
static __device__ __forceinline__ void wfrag(const LAS char* p0, const LAS char* p1, WFrag& f) {
#pragma unroll
    for (int s = 0; s < 4; ++s) {
        const s16x4 a00 = __builtin_amdgcn_ds_read_tr16_b64_v4i16((LAS s16x4*)(p0 + s * 8192));
        const s16x4 a01 = __builtin_amdgcn_ds_read_tr16_b64_v4i16((LAS s16x4*)(p0 + s * 8192 + 1024));
        const s16x4 a10 = __builtin_amdgcn_ds_read_tr16_b64_v4i16((LAS s16x4*)(p1 + s * 8192));
        const s16x4 a11 = __builtin_amdgcn_ds_read_tr16_b64_v4i16((LAS s16x4*)(p1 + s * 8192 + 1024));
        f.a0[s] = __builtin_bit_cast(bf16x8, __builtin_shufflevector(a00, a01, 0, 1, 2, 3, 4, 5, 6, 7));
        f.a1[s] = __builtin_bit_cast(bf16x8, __builtin_shufflevector(a10, a11, 0, 1, 2, 3, 4, 5, 6, 7));
    }
}
static __device__ __forceinline__ void wgemm(const WFrag& f, const bf16x8 (&bf)[4], f32x4& o0, f32x4& o1) {
    o0 = (f32x4){0.f, 0.f, 0.f, 0.f}; o1 = o0;
#pragma unroll
    for (int s = 0; s < 4; ++s) {
        o0 = __builtin_amdgcn_mfma_f32_16x16x32_bf16(f.a0[s], bf[s], o0, 0, 0, 0);
        o1 = __builtin_amdgcn_mfma_f32_16x16x32_bf16(f.a1[s], bf[s], o1, 0, 0, 0);
    }
}
static __device__ __forceinline__ u32x4 srd(const void* p, u32 bytes) {
    const uint64_t a = (uint64_t)p;
    return (u32x4){(u32)a, (u32)(a >> 32) & 0xffffu, bytes, 0x00020000u};
}
static __device__ __forceinline__ void load_w(u32x4 (&v)[4], u32x4 r, u32 va, u32 vb) {
    asm volatile("buffer_load_dwordx4 %0, %4, %6, 0 offen\n\tbuffer_load_dwordx4 %1, %4, %6, 0 offen offset:1024\n\t"
                 "buffer_load_dwordx4 %2, %5, %6, 0 offen offset:2048\n\tbuffer_load_dwordx4 %3, %5, %6, 0 offen offset:3072"
                 : "=&v"(v[0]), "=&v"(v[1]), "=&v"(v[2]), "=&v"(v[3]) : "v"(va), "v"(vb), "s"(r));
}

static __device__ __forceinline__ u32x4 load_piece(__amdgpu_buffer_rsrc_t rw, u32 voff, u32 soff) {
    asm volatile("" ::: "memory");
    return __builtin_amdgcn_raw_buffer_load_b128(rw, voff, soff, 16);
}
static __device__ __forceinline__ u32x4 poll_piece(u32x4 v, __amdgpu_buffer_rsrc_t rw, u32 voff, u32 soff, u32x4 tg) {
    for (int spin = 0; spin < (1 << 20); ++spin) {
        const u32 bad = ((v[0] ^ tg[0]) & TAG0) | (((v[1] ^ tg[1]) | (v[2] ^ tg[2]) | (v[3] ^ tg[3])) & TAG);
        if (bad == 0) break;
        if (POLL_SLEEP) __builtin_amdgcn_s_sleep(POLL_SLEEP);
        v = load_piece(rw, voff, soff);
    }
    return v;
}
static __device__ __forceinline__ u32 and_or(u32 a, u32 m, u32 c) { return __builtin_amdgcn_bitop3_b32(a, m, c, 0xEA); }
#define PIN(...) asm volatile("" : __VA_ARGS__)

__global__ __launch_bounds__(1024) void k_graph(const char* __restrict__ x, const int* __restrict__ src, const int* __restrict__ dst,
                                                const char* __restrict__ wq, const char* __restrict__ wv,
                                                char* __restrict__ out, char* ws, int N, int E
) {
    __shared__ __attribute__((aligned(16))) char smem_[L_END];
    LAS char* smem = (LAS char*)smem_;
    const int tid = threadIdx.x, lane = tid & 63;
    const int w = __builtin_amdgcn_readfirstlane(tid >> 6);
    const int b = blockIdx.x;
    u32 pf = 0;
    if (w == 7) {
        uint64_t pc; asm volatile("s_getpc_b64 %0" : "=s"(pc));
        asm volatile("global_load_dword %0, %1, %2" : "=v"(pf) : "v"(min(lane, CODE_LINES - 1) * 128), "s"(pc));
    }
    asm volatile("" :: "s"(x), "s"(src), "s"(dst), "s"(wq), "s"(wv), "s"(out), "s"(ws), "s"(N), "s"(E));
    ST_DECL;
    ST(0);
    const int CH = ((E + NB * 4 - 1) >> 10) * 4;
    const auto rw = __builtin_amdgcn_make_buffer_rsrc((void*)ws, 0, WS_END, 0x00020000);

    const u32x4 none = {NONE, NONE, NONE, NONE};
    const u32 t16 = tid * 16;
    const int ne = min(CH, E - b * CH);
    const bool binner = w * 256 < ne;
    u32x4 d4, s4;
    if (binner)
        asm volatile("buffer_load_dwordx4 %0, %2, %3, 0 offen" EDGE_MOD "\n\tbuffer_load_dwordx4 %1, %2, %4, 0 offen" EDGE_MOD
                     : "=&v"(d4), "=&v"(s4) : "v"(t16), "s"(srd(dst + b * CH, ne * 4)), "s"(srd(src + b * CH, ne * 4)));
    const u32x4 old = *(const __attribute__((address_space(4))) u32x4*)ws;
    const u32 t2 = tid >> 2, k16 = t16 & 48;
    u32 stg = t2 * STG_S + k16, poff = (t2 << 14) | k16, hoff = tid & 0x3fc, four = 4;
    *(LAS u32x4*)(smem + L_STG + stg) = none;
    if (w < 4) { *(LAS u32*)(smem + L_HIST + tid * 4) = 0; if (w == 0) { *(LAS u32*)(smem + L_CNT + tid * 4) = 0; *(LAS u32*)(smem + L_ARR + tid * 4) = 0; *(LAS u32*)(smem + L_ARR + 256 + (tid & 15) * 4) = 0; } }
    __syncthreads();
    const u32 c16 = t16 & 0xf0, g16 = tid & 48;
    u32x4 wr[4], xr0, xr1;
    if (w >= 8) {
        const int v = w - 8;
        const u32 wva = (c16 ^ (g16 << 2)) + (g16 << 4) + v * 4096;
        load_w(wr, srd(wq, 32768), wva, wva ^ 16);
        const u32 xa = ((c16 ^ g16) ^ ((v & 1) << 7)) + (g16 << 12) + (v << 19);
        if (v * 2048 + b < N)
            asm volatile("buffer_load_dwordx4 %0, %2, %4, 0 offen\n\tbuffer_load_dwordx4 %1, %3, %4, 0 offen"
                         : "=&v"(xr0), "=&v"(xr1) : "v"(xa), "v"((xa ^ 64) + (4 << 16)), "s"(srd(x + b * 256, (N - b) * 256)));
    }
    u32 xp0 = 0, xp1 = 0;
    auto xtouch = [&]() {
        const int v = w - 8, b2 = (b & ~7) | ((b + 1 + (v >> 1)) & 7);
        asm volatile("buffer_load_dword %0, %1, %2, 0 offen" : "=v"(xp0) : "v"(v < 2 * XPF_N ? lane << 16 : -4), "s"(srd(x + b2 * 256 + (v & 1) * 128, (N - b2) * 256 - 128)));
    };
    if (XPF == 1 && w >= 8) xtouch();
    u32x4 tg;
    {
        const uint64_t c0 = (((uint64_t)(old[0] | ~TAG0) << 32) | (old[1] | ~TAG)) + 1, c1 = (((uint64_t)(old[2] | ~TAG) << 32) | (old[3] | ~TAG)) + 1;
        tg[0] = (u32)(c0 >> 32) & TAG0; tg[1] = (u32)c0 & TAG; tg[2] = (u32)(c1 >> 32) & TAG; tg[3] = (u32)c1 & TAG;
    }
    const int nt = w & 3, cg = w >> 2;
    u32 wvp = ((c16 ^ (tid & 16) ^ (cg << 6)) | ((tid & 12) << 6) | (g16 << 7)) + L_WQ;
    u32 wq0 = wvp | ((tid & 1) << 3), wq1 = wq0 ^ 8;
    u32 xsp = ((c16 << 4) | (c16 ^ g16)) + (L_XS + nt * 4096);
    u32 agr = (lane & 15) * ROW_S + g16 + (L_Q + nt * 16 * ROW_S);
    u32 qw0 = agr + ((tid & 16) >> 1) + cg * 64, qw1 = qw0 ^ 8;
    u32 pso = WS_A + (((b & 7) << 5) | (b >> 3)) * 64;
    PIN("+s"(tg), "+s"(pso), "+v"(poff), "+v"(hoff), "+v"(stg), "+v"(four), "+v"(wvp), "+v"(wq0), "+v"(wq1), "+v"(xsp), "+v"(qw0), "+v"(qw1), "+v"(agr));
    if (binner) {
        asm volatile("s_waitcnt vmcnt(0)" : "+v"(d4), "+v"(s4));
        if (tid * 4 < ne) {
            u32 rank[4];
#pragma unroll
            for (int j = 0; j < 4; ++j) rank[j] = lds_add((LAS u32*)(smem + L_HIST + (d4[j] & 255) * 4), four);
#pragma unroll
            for (int j = 0; j < 4; ++j)
                *(LAS u32*)(smem + L_STG + (d4[j] & 255) * STG_S + min(rank[j], 128u)) = (s4[j] << 8) | ((d4[j] >> 6) & NMASK);
        }
    }
    ST(1);
    __syncthreads();
    ST(2);
    {
        const u32 c4 = *(const LAS u32*)(smem + L_HIST + hoff);
        u32x4 v = *(const LAS u32x4*)(smem + L_STG + stg) | tg;
        if (__builtin_expect(c4 > 64, 0)) {
            v[0] |= c4 > 128 ? F_OVER : F_MORE;
            u32x4 v2 = *(const LAS u32x4*)(smem + L_STG + stg + 64);
#pragma unroll
            for (int j = 0; j < 4; ++j) v2[j] = (64 + k16 + j * 4 < c4 ? v2[j] : NONE) | tg[j];
            __builtin_amdgcn_raw_buffer_store_b128(v2, rw, poff + (WS_B - WS_A), pso, 16);
        }
        __builtin_amdgcn_raw_buffer_store_b128(v, rw, poff, pso, 16);
    }
    ST(3);
    if (w >= 8) {
        if (XPF == 2) xtouch();
        if (XPF == 1 || XPF == 2) {
            asm volatile("s_waitcnt vmcnt(2)");
            PIN("+v"(wr[0]), "+v"(wr[1]), "+v"(wr[2]), "+v"(wr[3]), "+v"(xr0), "+v"(xr1));
        } else
        asm volatile("s_waitcnt vmcnt(1)"
                     : "+v"(wr[0]), "+v"(wr[1]), "+v"(wr[2]), "+v"(wr[3]), "+v"(xr0), "+v"(xr1));
        LAS char* p = smem + (L_WQ - 8 * 4096) + (w * 4096 + (t16 & 0x3f0));
#pragma unroll
        for (int j = 0; j < 4; ++j) *(LAS u32x4*)(p + j * 1024) = wr[j];
        p = smem + (L_XS - 8 * 2048) + (w * 2048 + (t16 & 0x3f0));
        *(LAS u32x4*)p = xr0; *(LAS u32x4*)(p + 1024) = xr1;
    } else {
        *(LAS u32x4*)(smem + L_LIST + t16) = none; *(LAS u32x4*)(smem + L_LIST + 8192 + t16) = none;
        asm volatile("s_waitcnt vmcnt(1)" : "+v"(pf));
    }
    __syncthreads();
    ST(6);
    {
        WFrag f; f32x4 o0, o1; bf16x8 bf[4];
#pragma unroll
        for (int s = 0; s < 4; ++s) bf[s] = *(const LAS bf16x8*)(smem + (xsp ^ (s << 6)));
        wfrag(smem + wq0, smem + wq1, f);
        wgemm(f, bf, o0, o1);
        const float sc = -0.08838834764831845f * 1.4426950408889634f;
        o0 *= sc; o1 *= sc;
        *(LAS u32x2*)(smem + qw0) = (u32x2){pk_bf16(o0[0], o0[1]), pk_bf16(o0[2], o0[3])};
        *(LAS u32x2*)(smem + qw1) = (u32x2){pk_bf16(o1[0], o1[1]), pk_bf16(o1[2], o1[3])};
    }
    ST(4);
    if (XPF == 3 && w >= 8) xtouch();
    const u32x4 first = load_piece(rw, t16, WS_A + b * (NB * 64));
    __builtin_amdgcn_sched_barrier(0);
    u32x4 mk;
    {
        const uint64_t m = 0xffffull << (t16 & 48);
        const u32 up = __builtin_amdgcn_sbfe(tid, 2, 1);
        mk[0] = (u32)m & ~up; mk[1] = (u32)(m >> 32) & ~up; mk[2] = (u32)m & up; mk[3] = (u32)(m >> 32) & up;
    }
    u32 loff = t16 & 0x70, r4 = t2 & 12, h4 = (tid >> 1) & 4, emask = EMASK, nmask = NMASK;
    u32 slb = h4 * (SLOT_S / 4) + ((tid & 16) << 4) - loff + (L_SLOT + w * 2 * SLOT_S);
    u32 agw = t16 + (tid & 0x3f0), ooff = (c16 << 12) | g16, l4 = lane * 4, sgi = t2 & 0xfc;
    u32 sub = ~tid & 32, lrow = lane * ((ROW_S << 16) + 256) + sub;
    u32 sla = (h4 << 6) + r4 + (L_LIST + w * 512), srow = h4 * (ROW_S / 4) + loff + w * 2 * ROW_S;
    u32 vva = ((c16 ^ (g16 << 2)) ^ ((w & 1) << 4)) + (g16 << 4) + w * 2048, vvp = (t16 & 0x3f0) + (L_WV + w * 2048);
    PIN("+v"(mk), "+v"(loff), "+v"(r4), "+v"(h4), "+v"(emask), "+v"(nmask), "+v"(slb));
    PIN("+v"(agw), "+v"(ooff), "+v"(l4), "+v"(lrow), "+v"(sgi), "+v"(sub), "+v"(sla), "+v"(srow), "+v"(vva), "+v"(vvp));
    const auto rx = __builtin_amdgcn_make_buffer_rsrc((void*)x, 0, N * 256, 0x00020000);
    auto append = [&](const u32x4 e) {
        u32 r[4], n4[4];
#pragma unroll
        for (int j = 0; j < 4; ++j) {
            n4[j] = e[j] & nmask;
            asm("" : "=v"(r[j]));
            if (n4[j] != NMASK) r[j] = lds_add((LAS u32*)(smem + L_CNT + n4[j]), four);
        }
#pragma unroll
        for (int j = 0; j < 4; ++j) *(LAS u32*)(smem + L_LIST + and_or(r[j], nmask, n4[j] << 6)) = e[j];
    };
    u32x4 vr0, vr1;
    {
        u32x4 ea = poll_piece(first, rw, t16, WS_A + b * (NB * 64), tg);
        PIN("+v"(ea), "+v"(xp0), "+v"(xp1));
        ST(5);
        asm volatile("buffer_load_dwordx4 %0, %2, %3, 0 offen\n\tbuffer_load_dwordx4 %1, %2, %3, 0 offen offset:1024"
                     : "=&v"(vr0), "=&v"(vr1) : "v"(vva), "s"(srd(wv, 32768)));
        append(ea);
        if (__builtin_expect((ea[0] & (F_MORE | F_OVER)) != 0, 0)) {
            if (ea[0] & F_OVER) lds_add((LAS u32*)(smem + L_CNT + NMASK), 4096u);
            else append(poll_piece(load_piece(rw, t16, WS_B + b * (NB * 64)), rw, t16, WS_B + b * (NB * 64), tg));
        }
    }
    __syncthreads();
    __builtin_amdgcn_s_setprio(3);
    ST(7);
    const u32 c4 = *(const LAS u32*)(smem + L_CNT + l4);
    f32x4 tot0 = {0.f, 0.f, 0.f, 0.f}, tot1 = tot0;
    WFrag wf;
    u32 aa, ab, ea, eb, rowa, rowb;
    auto fetch = [&](Quad& A, Quad& B) {
        const u32 oa = and_or(ea, emask, loff), ob = and_or(eb, emask, loff);
        A.x0 = __builtin_amdgcn_raw_buffer_load_b128(rx, oa, 0, 0); A.x1 = __builtin_amdgcn_raw_buffer_load_b128(rx, oa + 128, 0, 0);
        B.x0 = __builtin_amdgcn_raw_buffer_load_b128(rx, ob, 0, 0); B.x1 = __builtin_amdgcn_raw_buffer_load_b128(rx, ob + 128, 0, 0);
        __builtin_amdgcn_sched_barrier(0);
        A.row = rowa; B.row = rowb;
        A.q0 = *(const LAS u32x4*)(smem + L_Q + rowa); A.q1 = *(const LAS u32x4*)(smem + L_Q + 128 + rowa);
        B.q0 = *(const LAS u32x4*)(smem + L_Q + rowb); B.q1 = *(const LAS u32x4*)(smem + L_Q + 128 + rowb);
    };
    Quad A0, B0, A1, B1;
    ea = *(const LAS u32*)(smem + sla); eb = *(const LAS u32*)(smem + 16 + sla);
    rowa = rowb = srow;
    fetch(A0, B0);
    ST(8);
    asm volatile("s_waitcnt vmcnt(4)" : "+v"(vr0), "+v"(vr1));
    if (b == 0 && tid == 0) __builtin_amdgcn_raw_buffer_store_b128(tg, rw, 0, 0, 16);
    if (__builtin_expect(__ballot(c4 > 256) == 0, 1)) {
        A0.row = B0.row = FIX0 * ROW_S + loff;
        const u32 nq = (__builtin_elementwise_sub_sat(c4, sub) + 12) >> 4;
        const u32 inc = dpp_scan_incl(nq), exc = inc - nq;
        const int nbt = (__builtin_amdgcn_readlane(inc, 63) + 3) >> 2, na = nbt >> 4, nr = nbt & 15;
        const int nb = na + (w < nr), n = 2 * nb;
        const int q0 = 4 * (na * w + min(w, nr));
        const u32 pk = lrow - exc * 16;
        int mlo = __popcll(__ballot(exc <= (u32)q0)) - 1, mhi = __popcll(__ballot(exc < (u32)(q0 + 2 * n)));
        PIN("+s"(mlo), "+s"(mhi));
        const u32 q = q0 + lane;
        u32 off = __builtin_amdgcn_readlane(pk, mlo);
        for (int m = mlo + 1; m < mhi; ++m) {
            const u32 s = __builtin_amdgcn_readlane(exc, m), p = __builtin_amdgcn_readlane(pk, m);
            off = q >= s ? p : off;
        }
        const u32 sched = min(off + q * 16, 63u * ((ROW_S << 16) + 256) + 240);
        u32 idx = h4 * n;
        auto addr = [&](int o) { aa = __builtin_amdgcn_ds_bpermute(idx + o, sched); ab = __builtin_amdgcn_ds_bpermute(idx + o + 4, sched); };
        auto ents = [&]() {
            ea = *(const LAS u32*)(smem + L_LIST + ((aa & 0xffff) + r4)); eb = *(const LAS u32*)(smem + L_LIST + ((ab & 0xffff) + r4));
            rowa = (aa >> 16) + loff; rowb = (ab >> 16) + loff;
        };
        addr(0); ents(); addr(8);
        fetch(A1, B1);
        ents(); addr(16);
        __builtin_amdgcn_s_setprio(2);
        *(LAS u32x4*)(smem + vvp) = vr0; *(LAS u32x4*)(smem + vvp + 1024) = vr1;
        if (w == 15) {
            const float r1 = __builtin_amdgcn_rcpf((float)(2 * na + 2)), r0 = __builtin_amdgcn_rcpf((float)(2 * na));
            const u32 qs = 4 * nr * (na + 1);
            const float sh = (float)(2 * nr) - (float)qs * r0;
            const float f = (float)exc + 0.5f, l = (float)inc - 0.5f;
            const u32 gf = (u32)(exc < qs ? f * r1 : f * r0 + sh), gl = (u32)(inc <= qs ? l * r1 : l * r0 + sh);
            *(LAS u32*)(smem + L_SEG + l4) = nq > 0 ? (gf * SLOT_S) << 8 | (gl - gf + 1) : 0u;
        }
        asm volatile("" ::: "memory");
        if (lane == 0) lds_add((LAS u32*)(smem + L_ARR), 1u);
        const unsigned long long chg = __ballot((sched >> 16) != (dppu<0x138>(sched) >> 16));
        unsigned long long fl = (((chg | (chg >> n)) & 0xffffffffull) | 1) << 2;
        const bool mine = (lane >> 5) == ((lane >> 3) & 1);
        f32x4 acc0 = {0.f, 0.f, 0.f, 0.f}, acc1 = acc0;
        u32 cur = A0.row;
        auto flush = [&]() { LAS char* p = smem + (slb + cur * 2); *(LAS f32x4*)p = acc0; *(LAS f32x4*)(p + 128) = acc1; };
        auto add = [&](const Quad& A, const u32x4 am, bool check) {
            if (__builtin_expect(check, 0)) {
                asm volatile("");
                if (mine && A.row != cur) { flush(); acc0 = (f32x4){0.f, 0.f, 0.f, 0.f}; acc1 = acc0; }
            }
            cur = A.row;
            acc0 = mfma16(am, A.x0, acc0); acc1 = mfma16(am, A.x1, acc1);
        };
        auto step = [&](const Quad& A, const Quad& B) {
            const u32 a2 = alphas(A, B);
            const u32 up = dppu<0x114>(a2), dn = dppu<0x104>(a2);
            const u32x4 ma = {a2 & mk[0], a2 & mk[1], up & mk[2], up & mk[3]};
            const u32x4 mb = {dn & mk[0], dn & mk[1], a2 & mk[2], a2 & mk[3]};
            add(A, ma, fl & 1); add(B, mb, fl & 2);
            fl >>= 2;
        };
        int t = 1;
        for (; t + 1 < nb; t += 2) {
            step(A0, B0); fetch(A0, B0); ents(); addr(24);
            asm volatile("s_cmp_lt_i32 %0, " PRIO_A "\n\ts_cbranch_scc1 1f\n\ts_cmp_lt_i32 %0, " PRIO_B "\n\ts_setprio 1\n\ts_cbranch_scc1 1f\n\ts_setprio 0\n1:" :: "s"(t) : "scc");
            step(A1, B1); fetch(A1, B1); ents(); addr(32);
            idx += 16; PIN("+v"(idx));
        }
        u32 seg;
        auto tail = [&]() {
            while (__builtin_amdgcn_readfirstlane(*(const volatile LAS u32*)(smem + L_ARR)) < 16) __builtin_amdgcn_s_sleep(1);
            asm volatile("" ::: "memory");
            seg = *(const LAS u32*)(smem + L_SEG + sgi);
            wfrag(smem + wvp + (L_WV - L_WQ), smem + wvp + (L_WV - L_WQ) + 8, wf);
        };
        step(A0, B0);
        if (t < nb) { fetch(A0, B0); step(A1, B1); tail(); step(A0, B0); } else { tail(); step(A1, B1); }
        if (mine) flush();
        ST(9);
        const u32 cmb = L_SLOT + agw * 2 - (l4 & 60) * 4;
        const u32 ns = seg & 255, r0 = cmb + (seg >> 8);
        u32 p0 = w < 8 ? cmb + FIX0 * SLOT_S : L_ZERO, p1 = ns > 0 ? r0 : L_ZERO, p2 = ns > 1 ? r0 + SLOT_S : L_ZERO;
        PIN("+v"(p0), "+v"(p1), "+v"(p2));
        __syncthreads();
        ST(10);
        tot0 = *(const LAS f32x4*)(smem + p0) + *(const LAS f32x4*)(smem + p1) + *(const LAS f32x4*)(smem + p2);
        tot1 = *(const LAS f32x4*)(smem + p0 + 256) + *(const LAS f32x4*)(smem + p1 + 256) + *(const LAS f32x4*)(smem + p2 + 256);
        if (__builtin_expect(ns > 2, 0)) {
            const LAS char* r = smem + r0 + 2 * SLOT_S;
            for (u32 k = ns - 2; k > 0; --k, r += SLOT_S) { tot0 += *(const LAS f32x4*)r; tot1 += *(const LAS f32x4*)(r + 256); }
        }
    } else {
        *(LAS u32x4*)(smem + vvp) = vr0; *(LAS u32x4*)(smem + vvp + 1024) = vr1;
        const int g = lane >> 4, i16 = lane & 15;
        const auto rd = __builtin_amdgcn_make_buffer_rsrc((void*)dst, 0, E * 4, 0x00020000);
        const auto rs = __builtin_amdgcn_make_buffer_rsrc((void*)src, 0, E * 4, 0x00020000);
        const u32x4 qv = *(const LAS u32x4*)(smem + L_Q + (4 * w + g) * ROW_S + i16 * 16);
        for (int base = 0; base < E; base += 4096) {
            const int f0 = base + tid * 4;
            const u32x4 dd = __builtin_amdgcn_raw_buffer_load_b128(rd, f0 * 4, 0, 0), ss = __builtin_amdgcn_raw_buffer_load_b128(rs, f0 * 4, 0, 0);
            u32 e[4];
#pragma unroll
            for (int j = 0; j < 4; ++j) e[j] = (f0 < E && (dd[j] & 255) == (u32)b) ? ((ss[j] << 8) | ((dd[j] >> 6) & NMASK)) : NONE;
            for (;;) {
                __syncthreads();
                if (tid < 64) *(LAS u32*)(smem + L_CNT + tid * 4) = 0;
                __syncthreads();
#pragma unroll 1
                for (int j = 0; j < 4; ++j) {
                    const u32 ej = e[0];
                    u32 keep = ej;
                    if (ej != NONE) {
                        const u32 r = lds_add((LAS u32*)(smem + L_CNT + (ej & NMASK)), 4u);
                        if (r < 256) { *(LAS u32*)(smem + L_LIST + ((ej & NMASK) << 6) + r) = ej; keep = NONE; }
                    }
                    e[0] = e[1]; e[1] = e[2]; e[2] = e[3]; e[3] = keep;
                }
                __syncthreads();
                const u32 call = *(const LAS u32*)(smem + L_CNT + lane * 4);
                const u32 cn = min(*(const LAS u32*)(smem + L_CNT + (4 * w + g) * 4), 256u);
                for (u32 k = 0; k < cn; k += 4) {
                    const u32 ej = *(const LAS u32*)(smem + L_LIST + (4 * w + g) * 256 + k);
                    const u32x4 xv = __builtin_amdgcn_raw_buffer_load_b128(rx, (ej & EMASK) | (i16 * 16), 0, 0);
                    float xf[8], u = 0.f;
#pragma unroll
                    for (int j = 0; j < 4; ++j) {
                        xf[2 * j] = __builtin_bit_cast(float, xv[j] << 16); xf[2 * j + 1] = __builtin_bit_cast(float, xv[j] & 0xffff0000u);
                        u += xf[2 * j] * __builtin_bit_cast(float, qv[j] << 16) + xf[2 * j + 1] * __builtin_bit_cast(float, qv[j] & 0xffff0000u);
                    }
                    u += dppf<0x128>(u); u += dppf<0x124>(u); u += dppf<0x122>(u); u += dppf<0x121>(u);
                    const float al = __builtin_amdgcn_rcpf(1.0f + __builtin_amdgcn_exp2f(u));
#pragma unroll
                    for (int j = 0; j < 4; ++j) { tot0[j] += al * xf[j]; tot1[j] += al * xf[4 + j]; }
                }
                if (__ballot(call > 256) == 0) break;
            }
        }
        __syncthreads();
        wfrag(smem + wvp + (L_WV - L_WQ), smem + wvp + (L_WV - L_WQ) + 8, wf);
    }
    *(LAS u32x4*)(smem + L_AGG + agw) = (u32x4){pk_bf16(tot0[0], tot0[1]), pk_bf16(tot0[2], tot0[3]), pk_bf16(tot1[0], tot1[1]), pk_bf16(tot1[2], tot1[3])};
    __syncthreads();
    {
        f32x4 o0, o1; bf16x8 bf[4];
#pragma unroll
        for (int s = 0; s < 4; ++s) bf[s] = *(const LAS bf16x8*)(smem + agr + s * 64);
        wgemm(wf, bf, o0, o1);
        const u32x4 ov = {pk_bf16(o0[0], o0[1]), pk_bf16(o0[2], o0[3]), pk_bf16(o1[0], o1[1]), pk_bf16(o1[2], o1[3])};
        const auto ro = __builtin_amdgcn_make_buffer_rsrc((void*)out, 0, N * 256, 0x00020000);
        __builtin_amdgcn_raw_buffer_store_b128(ov, ro, ooff, (nt << 20) + b * 256 + cg * 64, OUT_AUX);
    }
    ST(11);
    ST_END(0, 11);
}

extern "C" void kernel_launch(void* const* d_in, const int* in_sizes, int n_in,
                              void* d_out, int out_size, void* d_ws, size_t ws_size,
                              hipStream_t stream) {
    const int N = in_sizes[0] / 128, E = in_sizes[1];
    k_graph<<<NB, 1024, 0, stream>>>((const char*)d_in[0], (const int*)d_in[1], (const int*)d_in[2], (const char*)d_in[3], (const char*)d_in[4],
                                     (char*)d_out, (char*)d_ws, N, E
    );
}
```
